```python
import torch

def _softmax(x, axis=-1):
    return torch.softmax(x, dim=axis)
def _gelu(x):
    return torch.nn.functional.gelu(x, approximate="tanh")
def _silu(x):
    return torch.nn.functional.silu(x)
def _sigmoid(x):
    return torch.sigmoid(x)
def _layernorm(x, axis=-1, eps=1e-5):
    m = x.mean(dim=axis, keepdim=True)
    v = x.var(dim=axis, keepdim=True, unbiased=False)
    return (x - m) / torch.sqrt(v + eps)
def _scatter_add(tgt, idx, src, axis=0):
    return tgt.clone().index_add_(axis, idx, src)
def _segment_sum(src, seg, n):
    out = torch.zeros((n,) + src.shape[1:], dtype=src.dtype)
    return out.index_add_(0, seg, src)
def _pad(x, pw):
    flat = tuple(v for pair in reversed(pw) for v in pair)
    return torch.nn.functional.pad(x, flat)

kernel_name = 'gated_mix__spectral_gfnet__mha_block__prompt'

def setup_inputs():

    _w = (lambda a: (lambda t: t.to(torch.bfloat16) if t.is_floating_point() else t)(torch.as_tensor(a)))
    def _A_setup():
        B, S, D = 1, 2048, 448
        Df = D // 2 + 1
        return {
            "x": _w((lambda *s: torch.randn(*s, dtype=torch.bfloat16) * 0.05)(B, S, D)),
            "w_r": _w((lambda *s: torch.randn(*s, dtype=torch.bfloat16) * 6.4)(S, Df) * 0.02),
            "w_i": _w((lambda *s: torch.randn(*s, dtype=torch.bfloat16) * 6.4)(S, Df) * 0.02),
        }
    _A_in = _A_setup()

    def _B_setup():
        B, S, D, H = 1, 2048, 448, 16
        return {
            "x": _w((lambda *s: torch.randn(*s, dtype=torch.bfloat16) * 0.05)(B, S, D)),
            "wqkv": _w((lambda *s: torch.randn(*s, dtype=torch.bfloat16) * 0.8)(D, 3 * D)),
            "wo": _w((lambda *s: torch.randn(*s, dtype=torch.bfloat16) * 0.05)(D, D)),
        }
    _B_in = _B_setup()

    return {
        "A_x": _A_in["x"],
        "A_w_r": _A_in["w_r"],
        "A_w_i": _A_in["w_i"],
        "B_x": _B_in["x"],
        "B_wqkv": _B_in["wqkv"],
        "B_wo": _B_in["wo"],
        "gate_w": _w((lambda *s: torch.randn(*s, dtype=torch.bfloat16) * 0.05)(448) * 16.0),
    }

def reference(A_x, A_w_r, A_w_i, B_x, B_wqkv, B_wo, gate_w):
    def _A_ref(x, w_r, w_i):
        B, S, D = x.shape
        X = (lambda x, axes=(-2,-1): torch.fft.rfft2(x.float(), dim=axes))(x, axes=(-2, -1))
        W = w_r + 1j * w_i
        X = X * W
        y = (lambda x, s, axes=(-2,-1): torch.fft.irfft2(x, s=s, dim=axes).to(torch.bfloat16))(X, (S, D), axes=(-2, -1))
        return y

    def _B_ref(x, wqkv, wo):
        B, S, D = x.shape
        H = 16
        Dh = D // H
        qkv = (lambda x, *d: x.permute(*d))((x @ wqkv).reshape(B, S, 3, H, Dh), 2, 0, 3, 1, 4)
        q, k, v = qkv[0], qkv[1], qkv[2]
        scores = torch.einsum("bhqd,bhkd->bhqk", q, k) * (Dh ** -0.5)
        p = _softmax(scores, axis=-1)
        o = torch.einsum("bhqk,bhkd->bhqd", p, v)
        return (lambda x, *d: x.permute(*d))(o, 0, 2, 1, 3).reshape(B, S, D) @ wo

    x = A_x
    g = _sigmoid((x * gate_w).sum(-1, keepdims=True))
    a = _A_ref(x, A_w_r, A_w_i)
    b = _B_ref(x, B_wqkv, B_wo)
    return g * a + (1 - g) * b
```

```cpp
#include <hip/hip_runtime.h>
#include <hip/hip_bf16.h>
#include <cstdint>

typedef unsigned short u16;
typedef __attribute__((ext_vector_type(8))) __bf16 bf16x8;
typedef __attribute__((ext_vector_type(2))) __bf16 bf16x2;
typedef __attribute__((ext_vector_type(16))) float f32x16;
typedef __attribute__((ext_vector_type(4))) float f32x4;
typedef __attribute__((ext_vector_type(2))) float f32x2;
typedef __attribute__((ext_vector_type(4))) unsigned u32x4;
typedef __attribute__((ext_vector_type(2))) unsigned u32x2;
typedef __attribute__((ext_vector_type(4))) short s16x4;
#define LAS __attribute__((address_space(3)))
#define GAS __attribute__((address_space(1)))
typedef LAS char* lptr;
typedef const GAS char* gcptr;

constexpr double PI_ = 3.14159265358979323846264338327950288;
constexpr double tsin(double x) { double x2 = x * x, t = x, s = x; for (int i = 1; i < 12; ++i) { t *= -x2 / ((2 * i) * (2 * i + 1)); s += t; } return s; }
constexpr double tcos(double x) { double x2 = x * x, t = 1, s = 1; for (int i = 1; i < 12; ++i) { t *= -x2 / ((2 * i - 1) * (2 * i)); s += t; } return s; }
constexpr double cosN(int m, int N) {
    m %= N; if (m < 0) m += N; if (2 * m > N) m = N - m; double sg = 1; if (4 * m > N) { m = N / 2 - m; sg = -1; }
    return (8 * m <= N) ? sg * tcos(2 * PI_ * m / N) : sg * tsin(2 * PI_ * (N / 4 - m) / N); }
constexpr double sinN(int m, int N) { return cosN(m - N / 4, N); }
constexpr u16 f2bf(double d) { float f = (float)d; uint32_t u = __builtin_bit_cast(uint32_t, f); return (u16)((u + 0x7FFFu + ((u >> 16) & 1u)) >> 16); }

struct Cos448 { u16 c[448]; };
constexpr Cos448 mk448(double sc) { Cos448 t{}; for (int i = 0; i < 448; ++i) t.c[i] = f2bf(sc * cosN(i, 448)); return t; }
static constexpr Cos448 C448 = mk448(1.0);
static constexpr Cos448 C448I = mk448(2.0 / (448.0 * 2048.0));
static constexpr Cos448 C448H = mk448(1.0 / (448.0 * 2048.0));
constexpr int plane_f(int p) { return p == 0 ? 0 : p == 1 ? 224 : p >> 1; }
constexpr int plane_ph(int p) { return (p >= 2 && (p & 1)) ? 112 : 0; }
struct FTile { alignas(16) u16 v[448 * 112]; };
constexpr FTile mkF(int ct) { FTile t{};
    for (int c = 0; c < 112; ++c) { const int p = ct * 112 + c, f = plane_f(p); int idx = plane_ph(p);
        for (int n = 0; n < 448; ++n) { t.v[n * 112 + c] = C448.c[idx]; idx += f; if (idx >= 448) idx -= 448; } }
    return t; }
static constexpr FTile FT0 = mkF(0), FT1 = mkF(1), FT2 = mkF(2), FT3 = mkF(3);
struct FAll { FTile t[4]; };
static __device__ const FAll c_F = {{FT0, FT1, FT2, FT3}};
struct GTile { alignas(16) u16 v[2 * 28 * 512]; };
constexpr GTile mkG(int w2) { GTile t{};
    for (int w = 0; w < 2; ++w) for (int p = 0; p < 448; ++p) { const int f = plane_f(p), j0 = 28 * (2 * w2 + w); int idx = (plane_ph(p) + f * j0) % 448;
        const int o = (w * 28 + (p >> 4)) * 512 + ((p >> 3) & 1) * 256 + (p & 7);
        if (p < 2) { for (int r = 0; r < 28; ++r) { t.v[o + r * 8] = C448H.c[idx]; idx += f; if (idx >= 448) idx -= 448; } }
        else { for (int r = 0; r < 28; ++r) { t.v[o + r * 8] = C448I.c[idx]; idx += f; if (idx >= 448) idx -= 448; } } }
    return t; }
static constexpr GTile GT0 = mkG(0), GT1 = mkG(1), GT2 = mkG(2), GT3 = mkG(3), GT4 = mkG(4), GT5 = mkG(5), GT6 = mkG(6), GT7 = mkG(7);
struct GAll { GTile t[8]; };
static __device__ const GAll c_G = {{GT0, GT1, GT2, GT3, GT4, GT5, GT6, GT7}};
struct FftTab {
    alignas(16) u16 t64[4][2][2][64][8];
    alignas(16) u16 t32[2][2][64][8];
    alignas(16) float tw[4][2][2][64][4];
};
constexpr FftTab mkFft() { FftTab t{};
    for (int w = 0; w < 4; ++w) for (int ks = 0; ks < 2; ++ks) for (int l = 0; l < 64; ++l) for (int e = 0; e < 8; ++e) {
        const int g = l >> 4, a = 32 * ks + 16 * (g >> 1) + 8 * (e >> 2) + 4 * (g & 1) + (e & 3), k1 = 16 * w + (l & 15);
        t.t64[w][0][ks][l][e] = C448.c[(7 * a * k1) % 448]; t.t64[w][1][ks][l][e] = C448.c[(7 * a * k1 + 336) % 448]; }
    for (int tt = 0; tt < 2; ++tt) for (int l = 0; l < 64; ++l) for (int e = 0; e < 8; ++e) {
        const int b = 16 * (e >> 2) + 4 * (l >> 4) + (e & 3), k2 = 16 * tt + (l & 15);
        t.t32[0][tt][l][e] = C448.c[(14 * b * k2) % 448]; t.t32[1][tt][l][e] = C448.c[(14 * b * k2 + 336) % 448]; }
    for (int w = 0; w < 4; ++w) for (int tt = 0; tt < 2; ++tt) for (int l = 0; l < 64; ++l) for (int r = 0; r < 4; ++r) {
        const int b = 16 * tt + 4 * (l >> 4) + r, k1 = 16 * w + (l & 15);
        t.tw[w][tt][0][l][r] = (float)cosN(b * k1, 2048); t.tw[w][tt][1][l][r] = (float)sinN(b * k1, 2048); }
    return t; }
static __device__ const FftTab c_T = mkFft();

constexpr unsigned WS_QF = 0;
constexpr unsigned WS_KV = WS_QF + (2u << 20);
constexpr unsigned WS_XT = WS_KV + (4u << 20);
constexpr unsigned WS_WT = WS_XT + 448 * 4096;
constexpr unsigned WS_YT = WS_WT + 256 * 8192;
constexpr unsigned WS_OT = WS_YT + 64 * 28672;
constexpr unsigned WS_WO = WS_OT + 64 * 28672;
constexpr unsigned WS_FA = WS_WO + 16 * 28672;
constexpr unsigned WS_FY = WS_FA + 8 * 256 * 128;
constexpr unsigned WS_FP = WS_FY + 2048;
constexpr unsigned WS_FO = WS_FP + 2048;
constexpr unsigned WS_EP = WS_FO + 1024 * 128;
constexpr unsigned WS_END = WS_EP + 128;

typedef unsigned long long u64;
typedef GAS u64 gu64;
#define RLX_AGENT __ATOMIC_RELAXED, __HIP_MEMORY_SCOPE_AGENT
#define MFMA32(a, b, c) __builtin_amdgcn_mfma_f32_32x32x16_bf16(a, b, c, 0, 0, 0)
#define MFMA16(a, b, c) __builtin_amdgcn_mfma_f32_16x16x32_bf16(a, b, c, 0, 0, 0)
#define SB() __builtin_amdgcn_sched_barrier(0)
#define PIN(x) asm volatile("" : "+v"(x))
#define EX(x) __builtin_amdgcn_exp2f(x)
#define ALL(p) (__builtin_amdgcn_ballot_w64(!(p)) == 0)
#define ANY(p) (__builtin_amdgcn_ballot_w64(p) != 0)
#define F2U(v) __builtin_bit_cast(unsigned, (float)(v))
#define U2F(v) __builtin_bit_cast(float, (unsigned)(v))
#define UMIN(a, b) __builtin_elementwise_min((unsigned)(a), (unsigned)(b))
#define UMAX(a, b) __builtin_elementwise_max((unsigned)(a), (unsigned)(b))
#define SWZ(v, x) U2F(__builtin_amdgcn_ds_swizzle((int)F2U(v), ((x) << 10) | 0x1F))
__device__ __forceinline__ unsigned pk(float a, float b) { f32x2 v = {a, b}; return __builtin_bit_cast(unsigned, __builtin_convertvector(v, bf16x2)); }
__device__ __forceinline__ bf16x8 asbf(u32x4 v) { return __builtin_bit_cast(bf16x8, v); }
__device__ __forceinline__ float bflo(unsigned v) { return U2F(v << 16); }
__device__ __forceinline__ float bfhi(unsigned v) { return U2F(v & 0xffff0000u); }
__device__ __forceinline__ bf16x8 lds128(lptr p) { return *(LAS bf16x8*)p; }
__device__ __forceinline__ bf16x8 ldstr(lptr p0, lptr p1) {
    u32x2 a = __builtin_bit_cast(u32x2, __builtin_amdgcn_ds_read_tr16_b64_v4i16((LAS s16x4*)p0));
    u32x2 b = __builtin_bit_cast(u32x2, __builtin_amdgcn_ds_read_tr16_b64_v4i16((LAS s16x4*)p1));
    return asbf((u32x4){a[0], a[1], b[0], b[1]}); }
typedef __amdgpu_buffer_rsrc_t rsrc_t;
__device__ __forceinline__ void st128(rsrc_t rs, unsigned off, u32x4 v) { __builtin_amdgcn_raw_buffer_store_b128(v, rs, off, 0, 16); }
__device__ __forceinline__ void st64(rsrc_t rs, unsigned off, u32x2 v) { __builtin_amdgcn_raw_buffer_store_b64(v, rs, off, 0, 16); }
__device__ __forceinline__ void st32(rsrc_t rs, unsigned off, unsigned v) { __builtin_amdgcn_raw_buffer_store_b32(v, rs, off, 0, 16); }
__device__ __forceinline__ u32x4 ld128(rsrc_t rs, unsigned off, unsigned soff) { return __builtin_amdgcn_raw_buffer_load_b128(rs, off, soff, 16); }
__device__ __forceinline__ unsigned ld32(rsrc_t rs, unsigned off) { return __builtin_amdgcn_raw_buffer_load_b32(rs, off, 0, 16); }
#define DMA_PRE "s_mov_b32 m0, %2\n\ts_nop 0\n\t"
#define DMA_OPS :: "v"(voff), "s"(sbase), "s"(ldst) : "memory"
__device__ __forceinline__ void dma1(const void* sbase, unsigned voff, unsigned ldst) { asm volatile(DMA_PRE "global_load_lds_dwordx4 %0, %1" DMA_OPS); }
__device__ __forceinline__ void dma1s(const void* sbase, unsigned voff, unsigned ldst) { asm volatile(DMA_PRE "global_load_lds_dwordx4 %0, %1 sc1" DMA_OPS); }
__device__ __forceinline__ void dma2s(const void* sbase, unsigned voff, unsigned ldst) {
    asm volatile(DMA_PRE "global_load_lds_dwordx4 %0, %1 sc1\n\tglobal_load_lds_dwordx4 %0, %1 offset:1024 sc1" DMA_OPS); }
__device__ __forceinline__ void dma4s(const void* sbase, unsigned voff, unsigned ldst) {
    asm volatile(DMA_PRE "global_load_lds_dwordx4 %0, %1 sc1\n\tglobal_load_lds_dwordx4 %0, %1 offset:1024 sc1\n\t"
                 "global_load_lds_dwordx4 %0, %1 offset:2048 sc1\n\tglobal_load_lds_dwordx4 %0, %1 offset:3072 sc1" DMA_OPS); }
__device__ __forceinline__ void dma7s(const char* src, unsigned voff, unsigned d) { dma4s(src, voff, d); dma2s(src + 4096, voff, d + 4096); dma1s(src + 6144, voff, d + 6144); }
template <int OFF> __device__ __forceinline__ void dmao(const void* sbase, unsigned voff, unsigned ldst) {
    asm volatile(DMA_PRE "global_load_lds_dwordx4 %0, %1 offset:%3 sc1" :: "v"(voff), "s"(sbase), "s"(ldst), "i"(OFF) : "memory"); }
#define WAITBAR(N) asm volatile("s_waitcnt vmcnt(" #N ") lgkmcnt(0)\n\ts_barrier" ::: "memory")
#define VBAR(N) asm volatile("s_waitcnt vmcnt(" #N ")\n\ts_barrier" ::: "memory")
#define LBAR() asm volatile("s_waitcnt lgkmcnt(0)\n\ts_barrier" ::: "memory")
#define DRAIN() asm volatile("s_waitcnt vmcnt(0)" ::: "memory")
__device__ __forceinline__ void poll(const char* ws, unsigned off, u64 tag) {
#pragma nounroll
    for (int spin = 0; spin < (1 << 18); ++spin) {
        if (ALL(__hip_atomic_load((gu64*)(ws + off), RLX_AGENT) == tag)) break;
    }
    asm volatile("" ::: "memory");
}

#define ST_DECL
#define ST(i)
#define ST_OUT()

#define CODE_KB 24
#define NO_OFLAG
#define PD1 17
#define PD2 31
#define OD 16
#define STAGGER (2 * ((idx + (idx >> 2)) & 3))
constexpr int P1_SLOT = 32768;
constexpr int L_RING = 0;
constexpr int L_Q = 131072;
constexpr int L_EX = L_Q + 8192;
constexpr int L_M = L_EX + 9216;
constexpr int L_XY = L_M + 512;
constexpr int L_FLG = L_XY + 8192;
constexpr int L_TAG = L_FLG + 2048;
constexpr int L_END = L_TAG + 16;
constexpr int L_Y0 = L_RING + 3 * 16384;
constexpr int L_Y1 = L_RING + 65536 + 16384;
constexpr int L_PART = L_Q;
constexpr int L_O = L_RING;
constexpr int L_A = L_RING + 65536 + 32768;
static_assert(L_PART + 16384 <= L_END, "lds");
constexpr int L_SJ = 131072;
static_assert(L_SJ + 4 * 6144 <= L_FLG, "lds");
__device__ __forceinline__ void dma4v(const void* sbase, unsigned v0, unsigned v1, unsigned v2, unsigned v3, unsigned ldst) {
    asm volatile("s_mov_b32 m0, %5\n\ts_nop 0\n\tglobal_load_lds_dwordx4 %0, %4\n\tglobal_load_lds_dwordx4 %1, %4 offset:1024\n\t"
                 "global_load_lds_dwordx4 %2, %4 offset:2048\n\tglobal_load_lds_dwordx4 %3, %4 offset:3072" :: "v"(v0), "v"(v1), "v"(v2), "v"(v3), "s"(sbase), "s"(ldst) : "memory"); }

template <bool SWAP>
__device__ __forceinline__ void p1_mma(lptr lds, int rh, int ch, int lane, f32x16 (&acc)[2][2]) {
    const int r = lane & 31, h = lane >> 5, sw = (r >> 1) & 7;
    const int blk = (lane >> 4) & 1, q = (lane & 15) >> 2, p = lane & 3;
    const int o0 = 112 * ch + 32 * blk + 8 * p, o1 = o0 + 56;
    lptr xb = lds + (64 * rh + r) * 128;
    lptr wa_ = lds + 16384 + h * 2048 + (o0 >> 6) * 256 + q * 64 + (o0 & 63);
    lptr wb_ = lds + 16384 + h * 2048 + (o1 >> 6) * 256 + q * 64 + (o1 & 63);
    int xo[4];
#pragma unroll
    for (int ks = 0; ks < 4; ++ks) xo[ks] = ((2 * ks + h) ^ sw) << 4;
    bf16x8 f[4][4];
#define LX0(slot, ks) f[ks][0] = lds128(xb + (slot) + xo[ks])
#define LX1(slot, ks) f[ks][1] = lds128(xb + (slot) + xo[ks] + 4096)
#define LWA(slot, ks) f[ks][2] = ldstr(wa_ + (slot) + (ks) * 4096, wa_ + (slot) + (ks) * 4096 + 1024)
#define LWB(slot, ks) f[ks][3] = ldstr(wb_ + (slot) + (ks) * 4096, wb_ + (slot) + (ks) * 4096 + 1024)
#define P1_MM(S, rt, cw) do { acc[rt][cw] = SWAP ? MFMA32(f[S][2 + cw], f[S][rt], acc[rt][cw]) : MFMA32(f[S][rt], f[S][2 + cw], acc[rt][cw]); SB(); } while (0)
#define P1_STEP(S, slot, ks) do { P1_MM(S, 0, 0); LX0(slot, ks); SB(); P1_MM(S, 1, 0); LWA(slot, ks); SB(); P1_MM(S, 0, 1); LX1(slot, ks); SB(); P1_MM(S, 1, 1); LWB(slot, ks); SB(); } while (0)
    int slot = 0;
    acc[0][0] = f32x16{}; acc[0][1] = f32x16{}; acc[1][0] = f32x16{}; acc[1][1] = f32x16{};
    PIN(acc[0][0]); PIN(acc[0][1]); PIN(acc[1][0]); PIN(acc[1][1]);
    VBAR(1);
    LX0(0, 0); SB(); LWA(0, 0); SB(); LX1(0, 0); SB(); LWB(0, 0); SB(); LX0(0, 1); SB(); LWA(0, 1); SB(); LX1(0, 1); SB(); LWB(0, 1); SB();
#pragma nounroll
    for (int c = 0; c < 7; ++c) {
        const int ns = (slot + P1_SLOT) & (4 * P1_SLOT - 1);
        P1_STEP(0, slot, 2); P1_STEP(1, slot, 3);
        if (c < 6) asm volatile("s_barrier" ::: "memory");
        P1_STEP(2, ns, 0); P1_STEP(3, ns, 1);
        slot = ns;
    }
#undef P1_STEP
#undef P1_MM
}

__device__ __forceinline__ float rowmax16(const f32x16& s) {
    float m = __builtin_fmaxf(__builtin_fmaxf(s[0], s[1]), s[2]);
#pragma unroll
    for (int i = 3; i < 15; i += 2) m = __builtin_fmaxf(__builtin_fmaxf(m, s[i]), s[i + 1]);
    m = __builtin_fmaxf(m, s[15]);
    auto rr = __builtin_amdgcn_permlane32_swap(F2U(m), F2U(m), false, false);
    return __builtin_fmaxf(U2F(rr[0]), U2F(rr[1]));
}
__device__ __forceinline__ bf16x8 negbf(bf16x8 v) { u32x4 u = __builtin_bit_cast(u32x4, v); u ^= 0x80008000u; return asbf(u); }
__device__ __forceinline__ bf16x8 pk8(const f32x4& a, const f32x4& b) { return asbf((u32x4){pk(a[0], a[1]), pk(a[2], a[3]), pk(b[0], b[1]), pk(b[2], b[3])}); }

#define EX4(S, i) S[i] = EX(S[i]); S[i + 1] = EX(S[i + 1]); S[i + 2] = EX(S[i + 2]); S[i + 3] = EX(S[i + 3]);
#define CV2(P, j, S, i) P[j] = pk(S[i], S[i + 1]); P[j + 1] = pk(S[i + 2], S[i + 3]); PIN(P);
#define STEP(SC, SN, VP, KP, VNP, H0, H1, H2) do { \
        SN = MFMA32(k0, q0, cinit); SB(); v1 = lds128((VP) + 3072); H0; SB(); EX4(SC, 0) CV2(p0, 0, SC, 0) SB(); \
        o = MFMA32(v1p, asbf(p1), o); SB(); k0 = lds128(KP); H1; SB(); EX4(SC, 4) CV2(p0, 2, SC, 4) SB(); \
        SN = MFMA32(k1, q1, SN); SB(); H2; SB(); EX4(SC, 8) CV2(p1, 0, SC, 8) SB(); \
        o = MFMA32(v0, asbf(p0), o); SB(); k1 = lds128((KP) + 1024); SB(); v0 = lds128((VNP) + 2048); SB(); EX4(SC, 12) CV2(p1, 2, SC, 12) v1p = v1; SB(); \
        } while (0)
#define PAIRH(A0, A1, A2, B0, B1, B2) do { const int nxt = (cur + 8192) & 65535; lptr cp = ring + cur, np = ring + nxt; \
        STEP(sa, sb, cp, np, cp + 4096, A0, A1, A2); STEP(sb, sa, cp + 4096, np + 4096, np, B0, B1, B2); cur = nxt; } while (0)
#define NOP (void)0
#define DH(i) dmao<(i) * 1024>(dsrc, lv, ddst)
#define QUAD() do { PAIRH(NOP, NOP, DH(0), NOP, NOP, DH(1)); PAIRH(NOP, NOP, DH(2), NOP, NOP, DH(3)); } while (0)
#define ATT_INIT() PIN(cinit); f32x16 o = {}, sb; SB(); k0 = lds128(ring + 4096); SB(); k1 = lds128(ring + 5120); SB(); \
        bf16x8 v0 = lds128(ring + 2048), v1p = asbf((u32x4){0, 0, 0, 0}), v1; SB(); u32x4 p0, p1 = {0, 0, 0, 0}; int cur = 0;
#define KV_SRC(g, c) (kvh + (size_t)(28 * (g) + 4 * (c) + qt) * 4096)
#define KV_DST(g, c) (lds0 + L_RING + (g) * 65536 + ((c) & 3) * 16384 + qt * 4096)
#define KV_ISSUE(g, c) dma4s(KV_SRC(g, c), lv, KV_DST(g, c))

__device__ __forceinline__ void check256(const char* src, lptr lds, unsigned lds0, unsigned lv, u64 tag) {
    const unsigned tl = (unsigned)tag, th = (unsigned)(tag >> 32);
#pragma nounroll
    for (int spin = 0; spin < (1 << 18); ++spin) {
        const u32x4 y0 = *(LAS u32x4*)(lds + L_FLG + lv), y1 = *(LAS u32x4*)(lds + L_FLG + 1024 + lv);
        if (ALL(y0[0] == tl && y0[1] == th && y0[2] == tl && y0[3] == th && y1[0] == tl && y1[1] == th && y1[2] == tl && y1[3] == th)) break;
        dma2s(src, lv, lds0 + L_FLG); DRAIN();
    }
}

__global__ __launch_bounds__(512, 2) void fused_kernel(const u16* __restrict__ x, const u16* __restrict__ wqkv, const u16* __restrict__ w_r, const u16* __restrict__ w_i,
                                                        const u16* __restrict__ wo, const u16* __restrict__ gate_w, char* ws, u16* __restrict__ out) {
    __shared__ __attribute__((aligned(16))) char smem[L_END];
    lptr lds = (lptr)smem;
    const unsigned lds0 = (unsigned)(uintptr_t)lds;
    const int tid = __builtin_amdgcn_workitem_id_x(), lane = tid & 63, wid = __builtin_amdgcn_readfirstlane(tid >> 6);
    const int bid = __builtin_amdgcn_workgroup_id_x(), g8 = bid & 7, idx = bid >> 3;
    const int qt = wid & 3;
    ST_DECL ST(0);
    const int rb1 = (g8 & 3) * 4 + (idx & 3), ct = (g8 >> 2) * 8 + (idx >> 2);
    const int ldw = ct < 12 ? 2688 : 224;
    const char* xb = (const char*)x + (2 * rb1 + (qt >> 1)) * 896 + (qt & 1) * (32 * 28672);
    const char* wb = (ct < 12 ? (const char*)wqkv + ct * 224 : (const char*)&c_F + (ct - 12) * 100352) + (16 * qt * ldw - 3072);
    unsigned xv[4], wv[4];
#pragma unroll
    for (int j = 0; j < 4; ++j) {
        xv[j] = (8 * j + (lane >> 3)) * 28672 + (((lane & 7) ^ ((lane >> 4) + 4 * (j & 1))) << 4) - j * 1024;
        wv[j] = (4 * j + ((lane >> 2) & 3)) * ldw + (lane >> 4) * 64 + (lane & 3) * 16 + (3 - j) * 1024;
    }
    const bool wact = lane < 48 || (lane & 2) == 0;
    int cc = STAGGER + (wid >> 2); cc = cc == 7 ? 0 : cc;
#define P1_ISSUE(c) do { const unsigned d = lds0 + ((c) & 3) * P1_SLOT + qt * 4096; dma4v(xb + cc * 128, xv[0], xv[1], xv[2], xv[3], d); \
        if (wact) dma4v(wb + 64 * cc * ldw, wv[0], wv[1], wv[2], wv[3], d + 16384); cc = cc == 6 ? 0 : cc + 1; } while (0)
    P1_ISSUE(wid >> 2);
    SB();
    ST(1);
    const rsrc_t rs = __builtin_amdgcn_make_buffer_rsrc(ws, 0, WS_END, 0x00020000);
    const unsigned lv = lane * 16;
    const int r = lane & 31, h = lane >> 5;
    const int head = 2 * g8 + (idx >> 4), rb = idx & 15, ut = rb * 4 + qt;
    const int ut3 = rb * 4 + (head >> 2), win = (head & 3) * 4 + qt;
    const char* kvh = ws + WS_KV + (size_t)head * 64 * 4096;
    lptr part = lds + L_PART + qt * 4096 + lv;
    lptr xch = lds + L_A + qt * 4096 + lv;
    if (wid >= 4) {
        const int L = qt;
        const int row0 = 2 * (bid & 31) + 256 * (bid >> 5);
        const int wsh = L == 3 ? 26 : 2;
        const unsigned vw = ((lane >> 3) & 3) * (64 * 450) + h * (450 - wsh) + (lane & 7) * 16;
        const int gw = bid * 4 + L, hf = gw < 896 ? gw : 895, wn = hf / 56, sl = hf - wn * 56, hp = sl / 7, pc = sl - hp * 7;
        const int ka = pc < 6 ? 28 * (2 * hp + (pc >= 3)) + (pc % 3 == 2 ? 16 : (pc % 3) * 4) : 56 * hp + 20, kd = pc < 6 ? 8 : 28;
        unsigned vo = (((lane >> 4) & 1) * kd + ((lane >> 2) & 3)) * 896 + ((lane & 3) == 3 ? 40 : (lane & 3) * 16);
        const u16* wop = wo + ka * 448 + 28 * wn;
        asm volatile("" : "+v"(vo), "+s"(wop));
        asm volatile("s_barrier" ::: "memory");
        ST(8);
        P1_ISSUE(2); VBAR(8);
        ST(10);
        P1_ISSUE(3); VBAR(8);
        ST(13);
        P1_ISSUE(4); VBAR(8);
        ST(14);
        P1_ISSUE(5); VBAR(8);
        P1_ISSUE(6); VBAR(8);
        ST(2);
        const unsigned sj = lds0 + L_SJ + L * 6144;
        dma1(w_r + row0 * 225 + 58 * L, vw, sj); dma1(w_i + row0 * 225 + 58 * L, vw, sj + 1024);
        dma1(wop, vo, sj + 2048);
        dma1(x + (bid * 8 + L * 2) * 448, lv, sj + 3072); dma1(x + (bid * 8 + L * 2) * 448 + 512, lv < 752 ? lv : 752, sj + 4096);
        dma1(gate_w, lv < 880 ? lv : 880, sj + 5120);
        VBAR(6);
        ST(3);
        u64 tag = __hip_atomic_load((gu64*)(ws + WS_EP), RLX_AGENT) + 1;
        PIN(tag);
        {
            const int h2 = lane & 1, fl = lane < 57 - 7 * (L == 3) ? lane : 57 - 7 * (L == 3);
            lptr sp = lds + L_SJ + L * 6144;
            unsigned wr[8], wi[8], e[8];
#pragma unroll
            for (int i = 0; i < 8; ++i) { lptr b = sp + i * 128 + fl * 2 + (i >> 2) * wsh; wr[i] = *(LAS u16*)b; wi[i] = *(LAS u16*)(b + 1024); }
#pragma unroll
            for (int i = 0; i < 8; ++i) e[i] = *(LAS u16*)(sp + 2048 + i * 64 + (r < 24 ? r * 2 : r < 28 ? r * 2 + 8 : 0));
            const int l = r < 28 ? r : 27;
            lptr xp = sp + 3072 + h * 896 + l * 32, gp = sp + 5120 + l * 32;
            const u32x4 a0 = *(LAS u32x4*)xp, a1 = *(LAS u32x4*)(xp + 16), g0 = *(LAS u32x4*)gp, g1 = *(LAS u32x4*)(gp + 16);
            u32x4 ev, od;
#pragma unroll
            for (int i = 0; i < 4; ++i) {
                const unsigned A = wr[i] | (wi[i] << 16), B = wr[4 + i] | (wi[4 + i] << 16);
                const unsigned got = __builtin_amdgcn_mov_dpp(h2 ? A : B, 0xB1, 0xF, 0xF, true);
                ev[i] = h2 ? got : A; od[i] = h2 ? B : got;
            }
            st128(rs, WS_WT + bid * 8192 + L * 1024 + lv, ev);
            const unsigned em = r < 28 ? ~0u : 0u;
            st128(rs, gw < 896 && !h ? WS_WO + gw * 512 + lv : 0x40000000u, (u32x4){(e[0] | (e[1] << 16)) & em, (e[2] | (e[3] << 16)) & em, (e[4] | (e[5] << 16)) & em, (e[6] | (e[7] << 16)) & em});
            float z = 0.f;
#pragma unroll
            for (int i = 0; i < 4; ++i) { z += bflo(a0[i]) * bflo(g0[i]); z += bfhi(a0[i]) * bfhi(g0[i]); z += bflo(a1[i]) * bflo(g1[i]); z += bfhi(a1[i]) * bfhi(g1[i]); }
            if (r >= 28) z = 0.f;
            z += SWZ(z, 16); z += SWZ(z, 8); z += SWZ(z, 4); z += SWZ(z, 2); z += SWZ(z, 1);
            const unsigned gt = F2U(__builtin_amdgcn_rcpf(1.f + EX(-1.4426950408889634f * z)));
            const auto g2 = __builtin_amdgcn_permlane32_swap(gt, gt, false, false);
            if (lane == 63) { od[0] = g2[0]; od[1] = g2[1]; }
            st128(rs, WS_WT + bid * 8192 + 4096 + L * 1024 + lv, od);
        }
        *(LAS u64*)(lds + L_TAG) = tag;
        ST(4);
        LBAR();
        ST(5);
        if (wid == 4) {
            st128(rs, WS_FA + ((lane >> 3) * 256 + ct * 16 + rb1) * 128 + (lane & 7) * 16, (u32x4){(unsigned)tag, (unsigned)(tag >> 32), 0u, 0u});
            const int pid = lane < 32 ? (4 + (lane >> 4) * 4 + (head >> 2)) * 16 + (lane & 15) : (head >> 2) * 16 + rb;
            if (((ct + 1) & 2) || ct >= 12) __builtin_amdgcn_s_sleep(PD1); else __builtin_amdgcn_s_sleep(PD2);
            poll(ws, WS_FA + (g8 * 256 + pid) * 128, tag);
        }
        asm volatile("s_barrier" ::: "memory");
        ST(6);
        lptr ring = lds + L_RING + 65536 + lv;
        dma2s(ws + WS_QF + (size_t)(head * 64 + ut) * 2048, lv, lds0 + L_Q + qt * 2048);
        KV_ISSUE(1, 0); KV_ISSUE(1, 1);
        VBAR(4);
        ST(7);
        const bf16x8 q0 = lds128(lds + L_Q + qt * 2048 + lv), q1 = lds128(lds + L_Q + qt * 2048 + 1024 + lv);
        bf16x8 k0 = lds128(ring), k1 = lds128(ring + 1024);
        f32x16 cinit, sa = {};
        sa = MFMA32(k0, q0, sa); sa = MFMA32(k1, q1, sa);
        const float m = rowmax16(sa);
        if (lane < 32) *(LAS float*)(lds + L_M + qt * 128 + lane * 4) = m;
#pragma unroll
        for (int i = 0; i < 16; ++i) { sa[i] -= m; cinit[i] = -m; }
        ATT_INIT()
        ST(9);
        {
            const char* dsrc = KV_SRC(1, 2); const unsigned ddst = KV_DST(1, 2);
            PAIRH(NOP, DH(0), DH(1), NOP, DH(2), DH(3));
        }
        const char* ysrc = ws + WS_YT + (size_t)ut3 * 28672 + qt * 7168;
        const unsigned ydst = lds0 + (qt < 2 ? L_Y0 + qt * 7168 : L_Y1 + (qt - 2) * 7168);
#pragma nounroll
        for (int j = 0; j < 8; ++j) {
            if (j == 7) {
                if (wid == 4) {
                    asm volatile("s_waitcnt vmcnt(4)" ::: "memory");
                    check256(ws + WS_FY, lds, lds0, lv, tag);
                    if (bid == 0 && lane == 0) __hip_atomic_store((gu64*)(ws + WS_EP), tag, RLX_AGENT);
                }
                __builtin_amdgcn_s_setprio(1);
            }
            VBAR(4);
            if (j == 6 && wid == 4) dma2s(ws + WS_FY, lv, lds0 + L_FLG);
            const char* dsrc = j < 6 ? KV_SRC(1, j + 3) : j == 6 ? (const char*)&c_G + ((2 * g8) & 3) * 114688 + (idx * 7 + (qt >> 1) * 3) * 1024 : ysrc;
            const unsigned ddst = j < 6 ? KV_DST(1, j + 3) : j == 6 ? lds0 + L_XY : ydst;
            QUAD();
        }
        {
            const char* dsrc = ysrc + 4096; const unsigned ddst = ydst + 4096;
            PAIRH(DH(0), DH(1), DH(2), NOP, NOP, NOP);
        }
        o = MFMA32(v1p, asbf(p1), o);
        ST(11);
#pragma unroll
        for (int i = 0; i < 4; ++i) *(LAS f32x4*)(part + i * 1024) = (f32x4){o[4 * i], o[4 * i + 1], o[4 * i + 2], o[4 * i + 3]};
        __builtin_amdgcn_s_setprio(0);
        bf16x8 wf[28];
        gcptr wsrc = (gcptr)(uintptr_t)c_G.t[0].v + (size_t)win * 28672 + lv;
#pragma unroll
        for (int ks = 0; ks < 14; ++ks) { wf[ks] = *(const GAS bf16x8*)(wsrc + ks * 1024); SB(); }
        WAITBAR(14);
        SB();
#pragma unroll
        for (int ks = 14; ks < 28; ++ks) { wf[ks] = *(const GAS bf16x8*)(wsrc + ks * 1024); SB(); }
        f32x16 acc = {};
        const int yo = (8 * h + ((lane & 15) >> 2)) * 64 + ((lane >> 4) & 1) * 32 + (lane & 3) * 8;
#pragma unroll
        for (int ks = 0; ks < 28; ++ks) { lptr y = lds + (ks < 14 ? L_Y0 + ks * 1024 : L_Y1 + (ks - 14) * 1024) + yo; acc = MFMA32(wf[ks], ldstr(y, y + 256), acc); }
#pragma unroll
        for (int i = 0; i < 4; ++i) *(LAS f32x4*)(xch + i * 1024) = (f32x4){acc[4 * i], acc[4 * i + 1], acc[4 * i + 2], acc[4 * i + 3]};
        ST(12);
        LBAR();
        ST_OUT();
        return;
    }
    {
        const char* pc; asm volatile("s_getpc_b64 %0" : "=s"(pc));
        const char* wsrc = (wid == 0 ? pc + (idx < CODE_KB ? idx : 0) * 1024 : wid == 1 ? (const char*)&c_T + idx * 1024 : (const char*)&c_T + 32768 + (idx & 3) * 1024) + lv;
        if (wid == 3) {
            const int k = lane & 7, ro = (2 * (bid & 31) + 256 * (bid >> 5) + 192 * (lane >> 3 & 1)) * 450;
            wsrc = lane < 32 ? ws + WS_EP + 256 : k == 0 ? (const char*)w_r + ro : k == 1 ? (const char*)w_i + ro : k == 2 ? (const char*)wo + (lane >> 3 & 1) * 401392
                 : k == 3 ? (const char*)gate_w : k == 4 ? (const char*)&c_G + (lane >> 3 & 1) * 458736 : (const char*)out + (lane >> 3 & 3) * 611664;
        }
        asm volatile("s_mov_b32 m0, %1\n\ts_nop 0\n\tglobal_load_lds_dwordx4 %0, off" :: "v"(wsrc), "s"(lds0 + L_FLG) : "memory");
    }
    {
        const int rh = wid >> 1, ch = wid & 1;
        f32x16 acc[2][2];
        if (ct < 8) {
            p1_mma<true>(lds, rh, ch, lane, acc);
            ST(3);
            const float sc = ct < 4 ? 0.18898223650461362f * 1.4426950408889634f : 1.f;
#pragma unroll
            for (int rt = 0; rt < 2; ++rt)
#pragma unroll
                for (int cw = 0; cw < 2; ++cw) {
                    const int hd = (ct & 3) * 4 + 2 * ch + cw, u1 = rb1 * 4 + rh * 2 + rt;
                    const unsigned dst = (ct < 4 ? WS_QF + (hd * 64 + u1) * 2048 : WS_KV + (hd * 64 + u1) * 4096) + lv;
                    const f32x16 a = acc[rt][cw] * sc;
                    st128(rs, dst, (u32x4){pk(a[0], a[1]), pk(a[2], a[3]), pk(a[4], a[5]), pk(a[6], a[7])});
                    st128(rs, dst + 1024, (u32x4){pk(a[8], a[9]), pk(a[10], a[11]), h ? 0u : pk(a[12], a[13]), h ? 0u : pk(a[14], a[15])});
                }
        } else {
            p1_mma<false>(lds, rh, ch, lane, acc);
            ST(3);
#pragma unroll
            for (int rt = 0; rt < 2; ++rt)
#pragma unroll
                for (int cw = 0; cw < 2; ++cw) {
                    const f32x16 a = acc[rt][cw];
                    u32x4 p0 = {pk(a[0], a[1]), pk(a[2], a[3]), pk(a[4], a[5]), pk(a[6], a[7])};
                    u32x4 p1 = {pk(a[8], a[9]), pk(a[10], a[11]), pk(a[12], a[13]), pk(a[14], a[15])};
                    if (ct < 12) {
                        const int hd = (ct & 3) * 4 + 2 * ch + cw, u1 = rb1 * 4 + rh * 2 + rt;
                        const unsigned dst = WS_KV + (hd * 64 + u1) * 4096 + 2048 + lv;
                        if (r >= 28) { const unsigned c = r == 28 ? 0x3f803f80u : 0u; p0 = (u32x4){c, c, c, c}; p1 = p0; }
                        st128(rs, dst, p0); st128(rs, dst + 1024, p1);
                    } else if (r < 28) {
                        const int pl = cw * 28 + r, q8 = (4 * rt + h) ^ (rb1 & 7) ^ (pl & 7);
                        lptr d = lds + wid * 7168 + pl * 128;
                        *(LAS u32x4*)(d + q8 * 16) = p0; *(LAS u32x4*)(d + (q8 ^ 2) * 16) = p1;
                    }
                }
            if (ct >= 12) {
#pragma unroll
                for (int i = 0; i < 7; ++i) {
                    const int pl = i * 8 + (lane >> 3);
                    const u32x4 v = *(LAS u32x4*)(lds + wid * 7168 + pl * 128 + (((lane & 7) ^ (pl & 7)) << 4));
                    st128(rs, WS_XT + rb1 * 114688 + ((ct - 12) * 112 + ch * 56 + pl) * 256 + rh * 128 + (lane & 7) * 16, v);
                }
            }
        }
    }
    ST(4);
    WAITBAR(0);
    ST(5);
    const u64 tag = *(LAS u64*)(lds + L_TAG);
    asm volatile("s_barrier" ::: "memory");
    ST(6);
    asm volatile("s_barrier" ::: "memory");
    ST(7);
    if (wid == 0 && lane == 0) __hip_atomic_store((gu64*)(ws + WS_FP + bid * 8), tag, RLX_AGENT);
    KV_ISSUE(0, 0); KV_ISSUE(0, 1);
    VBAR(4);
    bf16x8 c64[2], s64[2], c32[2], s32[2]; f32x4 twc[2], tws[2];
#pragma unroll
    for (int i = 0; i < 2; ++i) {
        c64[i] = *(const bf16x8*)c_T.t64[qt][0][i][lane]; s64[i] = *(const bf16x8*)c_T.t64[qt][1][i][lane];
        c32[i] = *(const bf16x8*)c_T.t32[0][i][lane]; s32[i] = *(const bf16x8*)c_T.t32[1][i][lane];
        twc[i] = *(const f32x4*)c_T.tw[qt][i][0][lane]; tws[i] = *(const f32x4*)c_T.tw[qt][i][1][lane];
    }
    SB();
    lptr ring = lds + L_RING + lv;
    const bf16x8 q0 = lds128(lds + L_Q + qt * 2048 + lv), q1 = lds128(lds + L_Q + qt * 2048 + 1024 + lv);
    bf16x8 k0 = lds128(ring), k1 = lds128(ring + 1024);
    f32x16 cinit, sa;
    {
        const float m = *(LAS float*)(lds + L_M + qt * 128 + r * 4);
#pragma unroll
        for (int i = 0; i < 16; ++i) cinit[i] = -m;
    }
    sa = MFMA32(k0, q0, cinit); sa = MFMA32(k1, q1, sa);
    ATT_INIT()
    ST(9);
    {
        const char* dsrc = KV_SRC(0, 2); const unsigned ddst = KV_DST(0, 2);
        if (wid == 0) dma2s(ws + WS_FP, lv, lds0 + L_FLG);
        PAIRH(NOP, DH(0), DH(1), NOP, DH(2), DH(3));
    }
    if (wid == 0) { asm volatile("s_waitcnt vmcnt(4)" ::: "memory"); check256(ws + WS_FP, lds, lds0, lv, tag); }
    VBAR(4);
    const int f = g8 * 32 + idx;
    const bool do_fft = f < 225, cplx = f != 0 && f != 224;
    const int pr = f == 0 ? 0 : f == 224 ? 1 : f < 225 ? 2 * f : 0;
    const int c = lane & 15, g = lane >> 4;
    u32x4 wq[2];
    {
        const char* dsrc = KV_SRC(0, 3); const unsigned ddst = KV_DST(0, 3);
        const unsigned wo_ = (g * 32 + (c >> 1)) * 8192 + (c & 1) * 16, fq = f < 232 ? f / 58 : 0, fl = f < 232 ? f - 58 * fq : 0, wso = WS_WT + qt * 8 * 8192 + (fl & 1) * 4096 + fq * 1024 + (fl >> 1) * 32;
        const char* xsrc = ws + WS_XT + (qt & 1) * (8 * 114688) + (pr + (qt >> 1)) * 256;
        const unsigned xo_ = (lane >> 4) * 114688 + (lane & 15) * 16;
        PAIRH(dma1s(xsrc, xo_, lds0 + L_XY + qt * 2048), dma1s(xsrc + 4 * 114688, xo_, lds0 + L_XY + qt * 2048 + 1024), wq[0] = ld128(rs, wo_, wso), wq[1] = ld128(rs, wo_, wso + 128 * 8192), DH(0), DH(1));
        PAIRH(DH(2), DH(3), NOP, NOP, NOP, NOP);
    }
    VBAR(4);
    ST(8);
    const f32x4 z = {};
    bf16x8 n64[2];
    if (do_fft) {
        bf16x8 ar[2][2], ai[2][2], n32[2];
#pragma unroll
        for (int t = 0; t < 2; ++t)
#pragma unroll
            for (int ks = 0; ks < 2; ++ks) {
                lptr a = lds + L_XY + t * 2048 + c * 128 + (((4 * ks + g) ^ (c >> 1)) << 4);
                ar[t][ks] = lds128(a); ai[t][ks] = lds128(a + 4096);
                if (!cplx) ai[t][ks] = asbf((u32x4){0, 0, 0, 0});
            }
#pragma unroll
        for (int i = 0; i < 2; ++i) { n64[i] = negbf(s64[i]); n32[i] = negbf(s32[i]); }
        f32x4 dr[2], di[2], er[2], ei[2];
#pragma unroll
        for (int t = 0; t < 2; ++t) {
            dr[t] = MFMA16(ar[t][0], c64[0], z); dr[t] = MFMA16(ar[t][1], c64[1], dr[t]); dr[t] = MFMA16(ai[t][0], s64[0], dr[t]); dr[t] = MFMA16(ai[t][1], s64[1], dr[t]);
            di[t] = MFMA16(ai[t][0], c64[0], z); di[t] = MFMA16(ai[t][1], c64[1], di[t]); di[t] = MFMA16(ar[t][0], n64[0], di[t]); di[t] = MFMA16(ar[t][1], n64[1], di[t]);
        }
#pragma unroll
        for (int t = 0; t < 2; ++t) { er[t] = dr[t] * twc[t] + di[t] * tws[t]; ei[t] = di[t] * twc[t] - dr[t] * tws[t]; }
        bf16x8 br = pk8(er[0], er[1]), bi = pk8(ei[0], ei[1]);
#pragma unroll
        for (int t = 0; t < 2; ++t) {
            dr[t] = MFMA16(c32[t], br, z); dr[t] = MFMA16(s32[t], bi, dr[t]);
            di[t] = MFMA16(c32[t], bi, z); di[t] = MFMA16(n32[t], br, di[t]);
        }
#pragma unroll
        for (int t = 0; t < 2; ++t) {
            const f32x4 wr = {bflo(wq[t][0]), bflo(wq[t][1]), bflo(wq[t][2]), bflo(wq[t][3])}, wi = {bfhi(wq[t][0]), bfhi(wq[t][1]), bfhi(wq[t][2]), bfhi(wq[t][3])};
            er[t] = dr[t] * wr - di[t] * wi; ei[t] = dr[t] * wi + di[t] * wr;
        }
        br = pk8(er[0], er[1]); bi = pk8(ei[0], ei[1]);
#pragma unroll
        for (int t = 0; t < 2; ++t) {
            dr[t] = MFMA16(c32[t], br, z); dr[t] = MFMA16(n32[t], bi, dr[t]);
            di[t] = MFMA16(c32[t], bi, z); di[t] = MFMA16(s32[t], br, di[t]);
        }
#pragma unroll
        for (int t = 0; t < 2; ++t) {
            er[t] = dr[t] * twc[t] - di[t] * tws[t]; ei[t] = di[t] * twc[t] + dr[t] * tws[t];
            lptr e = lds + L_EX + (16 * qt + c) * 72 + t * 32 + g * 8;
            *(LAS u32x2*)e = (u32x2){pk(er[t][0], er[t][1]), pk(er[t][2], er[t][3])};
            *(LAS u32x2*)(e + 4608) = (u32x2){pk(ei[t][0], ei[t][1]), pk(ei[t][2], ei[t][3])};
        }
    }
    __builtin_amdgcn_s_waitcnt(0x0F70);
    SB();
    {
        const char* dsrc = KV_SRC(0, 4); const unsigned ddst = KV_DST(0, 4);
        PAIRH(NOP, NOP, DH(0), NOP, NOP, DH(1));
        asm volatile("s_barrier" ::: "memory");
        if (do_fft) {
            lptr e = lds + L_EX + (16 * (g >> 1) + 4 * (g & 1) + (c >> 2)) * 72 + (c & 3) * 8;
#pragma unroll
            for (int t = 0; t < 2; ++t) {
                bf16x8 ur[2], ui[2];
#pragma unroll
                for (int ks = 0; ks < 2; ++ks) {
                    ur[ks] = ldstr(e + ks * 2304 + t * 32, e + ks * 2304 + t * 32 + 576);
                    ui[ks] = ldstr(e + 4608 + ks * 2304 + t * 32, e + 4608 + ks * 2304 + t * 32 + 576);
                }
                f32x4 yr = MFMA16(c64[0], ur[0], z); yr = MFMA16(c64[1], ur[1], yr); yr = MFMA16(n64[0], ui[0], yr); yr = MFMA16(n64[1], ui[1], yr);
                f32x4 yi = MFMA16(c64[0], ui[0], z); yi = MFMA16(c64[1], ui[1], yi); yi = MFMA16(s64[0], ur[0], yi); yi = MFMA16(s64[1], ur[1], yi);
                lptr d = lds + L_XY + ((16 * t + c) * 2 + (qt >> 1)) * 128 + (g & 1) * 8;
                const int q8 = (2 * (qt & 1) + (g >> 1)) ^ (c & 7);
                *(LAS u32x2*)(d + q8 * 16) = (u32x2){pk(yr[0], yr[1]), pk(yr[2], yr[3])};
                *(LAS u32x2*)(d + (q8 ^ 4) * 16) = (u32x2){pk(yi[0], yi[1]), pk(yi[2], yi[3])};
            }
        }
        SB();
        PAIRH(NOP, NOP, DH(2), NOP, NOP, DH(3));
    }
    const unsigned odst = WS_OT + ut * 28672 + (head >> 1) * 3584 + r * 16;
#define O_STORE(A, B) do { st128(rs, odst + ((head & 1) * 3 + h) * 512, A); \
        if (h == 0) st128(rs, odst + ((head & 1) * 3 + 2) * 512, B); else st64(rs, odst + 6 * 512 + (head & 1) * 8, (u32x2){B[0], B[1]}); } while (0)
#pragma nounroll
    for (int j = 4; j < 6; ++j) {
        VBAR(4);
        if (j == 4) {
            const unsigned ydst = !do_fft || (!cplx && (lane & 4)) ? 0x40000000u : WS_YT + (qt * 16 + (lane >> 3)) * 28672 + pr * 64 + (lane & 7) * 16;
#pragma unroll
            for (int i = 0; i < 2; ++i) { const int ln = qt * 16 + i * 8 + (lane >> 3);
                st128(rs, ydst + i * (8 * 28672), *(LAS u32x4*)(lds + L_XY + ln * 128 + (((lane & 7) ^ ((ln >> 1) & 7)) << 4))); }
            const u32x4 sent = {~0u, ~0u, ~0u, ~0u};
            O_STORE(sent, sent);
        } else if (wid == 0 && lane == 0) __hip_atomic_store((gu64*)(ws + WS_FY + bid * 8), tag, RLX_AGENT);
        const char* dsrc = KV_SRC(0, j + 1); const unsigned ddst = KV_DST(0, j + 1);
        QUAD();
    }
    VBAR(4);
    ST(10);
    const int u3 = ut3 * 32 + r, s3 = (u3 & 63) * 32 + (u3 >> 6);
    const float gate = U2F(ld32(rs, WS_WT + (s3 >> 3) * 8192 + 4096 + ((s3 >> 1) & 3) * 1024 + 1008 + (s3 & 1) * 4));
    bf16x8 wf[28];
#define WLD(i) do { if ((i) < 28) wf[(i) < 28 ? (i) : 0] = asbf(ld128(rs, lv + ((i) % 7 & 3) * 1024, WS_WO + win * 28672 + ((qt + (i) / 7) & 3) * 7168 + ((i) % 7 >> 2) * 4096)); } while (0)
#pragma unroll
    for (int p = 0; p < 5; ++p) {
        if (p == 2) VBAR(12);
        PAIRH(WLD(6 * p), WLD(6 * p + 1), WLD(6 * p + 2), WLD(6 * p + 3), WLD(6 * p + 4), WLD(6 * p + 5));
    }
    o = MFMA32(v1p, asbf(p1), o);
    ST(11);
    LBAR();
#pragma unroll
    for (int i = 0; i < 4; ++i) { const f32x4 t = *(LAS f32x4*)(part + i * 1024); o[4 * i] += t[0]; o[4 * i + 1] += t[1]; o[4 * i + 2] += t[2]; o[4 * i + 3] += t[3]; }
    float l = U2F(__builtin_amdgcn_permlane32_swap(F2U(o[12]), F2U(o[12]), false, false)[1]);
    if (__builtin_expect(ANY(!(l <= 1073741824.f)), 0)) {
        const unsigned kb = WS_KV + head * 64 * 4096;
        const f32x16 z16 = {};
        float m = -INFINITY;
#pragma nounroll
        for (int t = 0; t < 64; ++t) {
            f32x16 s = MFMA32(asbf(ld128(rs, lv, kb + t * 4096)), q0, z16); s = MFMA32(asbf(ld128(rs, lv + 1024, kb + t * 4096)), q1, s);
            m = __builtin_fmaxf(m, rowmax16(s));
        }
#pragma unroll
        for (int i = 0; i < 16; ++i) { cinit[i] = -m; o[i] = 0.f; }
#pragma nounroll
        for (int t = 0; t < 64; ++t) {
            f32x16 s = MFMA32(asbf(ld128(rs, lv, kb + t * 4096)), q0, cinit); s = MFMA32(asbf(ld128(rs, lv + 1024, kb + t * 4096)), q1, s);
            EX4(s, 0) EX4(s, 4) EX4(s, 8) EX4(s, 12) CV2(p0, 0, s, 0) CV2(p0, 2, s, 4) CV2(p1, 0, s, 8) CV2(p1, 2, s, 12)
            o = MFMA32(asbf(ld128(rs, lv + 2048, kb + t * 4096)), asbf(p0), o); o = MFMA32(asbf(ld128(rs, lv + 3072, kb + t * 4096)), asbf(p1), o);
        }
        l = U2F(__builtin_amdgcn_permlane32_swap(F2U(o[12]), F2U(o[12]), false, false)[1]);
    }
    {
        o *= __builtin_amdgcn_rcpf(l);
        u32x4 oa = {pk(o[0], o[1]), pk(o[2], o[3]), pk(o[4], o[5]), pk(o[6], o[7])}, ob = {pk(o[8], o[9]), pk(o[10], o[11]), pk(o[12], o[13]), pk(o[14], o[15])};
#pragma unroll
        for (int i = 0; i < 4; ++i) { oa[i] = UMIN(oa[i], 0xfffffffeu); ob[i] = UMIN(ob[i], 0xfffffffeu); }
        O_STORE(oa, ob);
        if (lane == 0) __hip_atomic_store((gu64*)(ws + WS_FO + (ut * 16 + head) * 128), tag, RLX_AGENT);
    }
    ST(12);
    if (OD) __builtin_amdgcn_s_sleep(OD);
    ST(13);
    u32x4 of[7];
#pragma nounroll
    for (int spin = 0; spin < (1 << 18); ++spin) {
        dma7s(ws + WS_OT + (size_t)ut3 * 28672 + qt * 7168, lv, lds0 + L_O + qt * 7168);
        DRAIN();
        unsigned mx = 0;
#pragma unroll
        for (int i = 0; i < 7; ++i) { of[i] = *(LAS u32x4*)(lds + L_O + qt * 7168 + i * 1024 + lv); mx = UMAX(UMAX(mx, UMAX(of[i][0], of[i][1])), UMAX(of[i][2], of[i][3])); }
        if (ALL(mx != ~0u)) break;
    }
    __builtin_amdgcn_s_waitcnt(0x0F70);
    f32x16 acc = MFMA32(wf[0], asbf(of[0]), f32x16{}); SB();
    asm volatile("s_barrier" ::: "memory");
    ST(14);
    {
        lptr ob[3];
#pragma unroll
        for (int i = 0; i < 3; ++i) ob[i] = lds + L_O + ((qt + 1 + i) & 3) * 7168 + lv;
        of[0] = *(LAS u32x4*)ob[0]; SB();
#pragma unroll
        for (int i = 1; i < 28; ++i) { acc = MFMA32(wf[i], asbf(of[i % 7]), acc); SB(); if (i < 21) of[i % 7] = *(LAS u32x4*)(ob[i / 7] + (i % 7) * 1024); SB(); }
    }
    u16* dst = out + s3 * 448 + 28 * win + 4 * h;
#pragma unroll
    for (int gi = 0; gi < 4; ++gi) {
        const f32x4 a = *(LAS f32x4*)(xch + gi * 1024);
        float t[4];
#pragma unroll
        for (int i = 0; i < 4; ++i) t[i] = gate * a[i] + (1.f - gate) * acc[4 * gi + i];
        if (gi < 3 || h == 0) *(u32x2*)(dst + 8 * gi) = (u32x2){pk(t[0], t[1]), pk(t[2], t[3])};
    }
    ST_OUT();
}

extern "C" void kernel_launch(void* const* d_in, const int* in_sizes, int n_in, void* d_out, int out_size, void* d_ws, size_t ws_size, hipStream_t stream) {
    fused_kernel<<<256, 512, 0, stream>>>((const u16*)d_in[0], (const u16*)d_in[4], (const u16*)d_in[1], (const u16*)d_in[2], (const u16*)d_in[5], (const u16*)d_in[6],
                                          (char*)d_ws, (u16*)d_out);
}
```

```cpp
#include <hip/hip_runtime.h>
#include <hip/hip_bf16.h>
#include <cstdint>

typedef unsigned short u16;
typedef __attribute__((ext_vector_type(8))) __bf16 bf16x8;
typedef __attribute__((ext_vector_type(2))) __bf16 bf16x2;
typedef __attribute__((ext_vector_type(16))) float f32x16;
typedef __attribute__((ext_vector_type(4))) float f32x4;
typedef __attribute__((ext_vector_type(2))) float f32x2;
typedef __attribute__((ext_vector_type(4))) unsigned u32x4;
typedef __attribute__((ext_vector_type(2))) unsigned u32x2;
typedef __attribute__((ext_vector_type(4))) short s16x4;
#define LAS __attribute__((address_space(3)))
#define GAS __attribute__((address_space(1)))
typedef LAS char* lptr;
typedef const GAS char* gcptr;

constexpr double PI_ = 3.14159265358979323846264338327950288;
constexpr double tsin(double x) { double x2 = x * x, t = x, s = x; for (int i = 1; i < 12; ++i) { t *= -x2 / ((2 * i) * (2 * i + 1)); s += t; } return s; }
constexpr double tcos(double x) { double x2 = x * x, t = 1, s = 1; for (int i = 1; i < 12; ++i) { t *= -x2 / ((2 * i - 1) * (2 * i)); s += t; } return s; }
constexpr double cosN(int m, int N) {
    m %= N; if (m < 0) m += N; if (2 * m > N) m = N - m; double sg = 1; if (4 * m > N) { m = N / 2 - m; sg = -1; }
    return (8 * m <= N) ? sg * tcos(2 * PI_ * m / N) : sg * tsin(2 * PI_ * (N / 4 - m) / N); }
constexpr double sinN(int m, int N) { return cosN(m - N / 4, N); }
constexpr u16 f2bf(double d) { float f = (float)d; uint32_t u = __builtin_bit_cast(uint32_t, f); return (u16)((u + 0x7FFFu + ((u >> 16) & 1u)) >> 16); }

struct Cos448 { u16 c[448]; };
constexpr Cos448 mk448(double sc) { Cos448 t{}; for (int i = 0; i < 448; ++i) t.c[i] = f2bf(sc * cosN(i, 448)); return t; }
static constexpr Cos448 C448 = mk448(1.0);
static constexpr Cos448 C448I = mk448(2.0 / (448.0 * 2048.0));
static constexpr Cos448 C448H = mk448(1.0 / (448.0 * 2048.0));
constexpr int plane_f(int p) { return p == 0 ? 0 : p == 1 ? 224 : p >> 1; }
constexpr int plane_ph(int p) { return (p >= 2 && (p & 1)) ? 112 : 0; }
struct GTile { alignas(16) u16 v[2 * 28 * 512]; };
constexpr GTile mkF(int w2) { GTile t{};
    for (int w = 0; w < 2; ++w) for (int r = 0; r < 28; ++r) { const int p = 28 * (2 * w2 + w) + r, f = plane_f(p); int idx = plane_ph(p);
        for (int n = 0; n < 448; ++n) { t.v[(w * 28 + (n >> 4)) * 512 + ((n >> 3) & 1) * 256 + r * 8 + (n & 7)] = C448.c[idx]; idx += f; if (idx >= 448) idx -= 448; } }
    return t; }
static constexpr GTile FT0 = mkF(0), FT1 = mkF(1), FT2 = mkF(2), FT3 = mkF(3), FT4 = mkF(4), FT5 = mkF(5), FT6 = mkF(6), FT7 = mkF(7);
struct GAll { GTile t[8]; };
static __device__ const GAll c_F = {{FT0, FT1, FT2, FT3, FT4, FT5, FT6, FT7}};
constexpr GTile mkG(int w2) { GTile t{};
    for (int w = 0; w < 2; ++w) for (int p = 0; p < 448; ++p) { const int f = plane_f(p), j0 = 28 * (2 * w2 + w); int idx = (plane_ph(p) + f * j0) % 448;
        const int o = (w * 28 + (p >> 4)) * 512 + ((p >> 3) & 1) * 256 + (p & 7);
        if (p < 2) { for (int r = 0; r < 28; ++r) { t.v[o + r * 8] = C448H.c[idx]; idx += f; if (idx >= 448) idx -= 448; } }
        else { for (int r = 0; r < 28; ++r) { t.v[o + r * 8] = C448I.c[idx]; idx += f; if (idx >= 448) idx -= 448; } } }
    return t; }
static constexpr GTile GT0 = mkG(0), GT1 = mkG(1), GT2 = mkG(2), GT3 = mkG(3), GT4 = mkG(4), GT5 = mkG(5), GT6 = mkG(6), GT7 = mkG(7);
static __device__ const GAll c_G = {{GT0, GT1, GT2, GT3, GT4, GT5, GT6, GT7}};
struct FftTab {
    alignas(16) u16 t64[4][2][2][64][8];
    alignas(16) u16 t32[2][2][64][8];
    alignas(16) float tw[4][2][2][64][4];
};
constexpr FftTab mkFft() { FftTab t{};
    for (int w = 0; w < 4; ++w) for (int ks = 0; ks < 2; ++ks) for (int l = 0; l < 64; ++l) for (int e = 0; e < 8; ++e) {
        const int g = l >> 4, a = 32 * ks + 16 * (g >> 1) + 8 * (e >> 2) + 4 * (g & 1) + (e & 3), k1 = 16 * w + (l & 15);
        t.t64[w][0][ks][l][e] = C448.c[(7 * a * k1) % 448]; t.t64[w][1][ks][l][e] = C448.c[(7 * a * k1 + 336) % 448]; }
    for (int tt = 0; tt < 2; ++tt) for (int l = 0; l < 64; ++l) for (int e = 0; e < 8; ++e) {
        const int b = 16 * (e >> 2) + 4 * (l >> 4) + (e & 3), k2 = 16 * tt + (l & 15);
        t.t32[0][tt][l][e] = C448.c[(14 * b * k2) % 448]; t.t32[1][tt][l][e] = C448.c[(14 * b * k2 + 336) % 448]; }
    for (int w = 0; w < 4; ++w) for (int tt = 0; tt < 2; ++tt) for (int l = 0; l < 64; ++l) for (int r = 0; r < 4; ++r) {
        const int b = 16 * tt + 4 * (l >> 4) + r, k1 = 16 * w + (l & 15);
        t.tw[w][tt][0][l][r] = (float)cosN(b * k1, 2048); t.tw[w][tt][1][l][r] = (float)sinN(b * k1, 2048); }
    return t; }
static __device__ const FftTab c_T = mkFft();

constexpr unsigned WS_KV = 0;
constexpr unsigned WS_XT = WS_KV + (4u << 20);
constexpr unsigned WS_WT = WS_XT + 64 * 28672;
constexpr unsigned WS_YT = WS_WT + 256 * 8192;
constexpr unsigned WS_OT = WS_YT + 64 * 28672;
constexpr unsigned WS_WO = WS_OT + 64 * 28672;
constexpr unsigned WS_G  = WS_WO + 16 * 28672;
constexpr unsigned WS_FA = WS_G + 8192;
constexpr unsigned WS_FX = WS_FA + 4 * 256 * 128;
constexpr unsigned WS_FY = WS_FX + 8192;
constexpr unsigned WS_EP = WS_FY + 8192;
constexpr unsigned WS_END = WS_EP + 128;

typedef unsigned long long u64;
typedef GAS u64 gu64;
#define RLX_AGENT __ATOMIC_RELAXED, __HIP_MEMORY_SCOPE_AGENT
#define MFMA32(a, b, c) __builtin_amdgcn_mfma_f32_32x32x16_bf16(a, b, c, 0, 0, 0)
#define MFMA16(a, b, c) __builtin_amdgcn_mfma_f32_16x16x32_bf16(a, b, c, 0, 0, 0)
#define SB() __builtin_amdgcn_sched_barrier(0)
#define PIN(x) asm volatile("" : "+v"(x))
#define EX(x) __builtin_amdgcn_exp2f(x)
#define ALL(p) (__builtin_amdgcn_ballot_w64(!(p)) == 0)
#define ANY(p) (__builtin_amdgcn_ballot_w64(p) != 0)
#define F2U(v) __builtin_bit_cast(unsigned, (float)(v))
#define U2F(v) __builtin_bit_cast(float, (unsigned)(v))
#define UMIN(a, b) __builtin_elementwise_min((unsigned)(a), (unsigned)(b))
#define UMAX(a, b) __builtin_elementwise_max((unsigned)(a), (unsigned)(b))
#define SWZ(v, x) U2F(__builtin_amdgcn_ds_swizzle((int)F2U(v), ((x) << 10) | 0x1F))
__device__ __forceinline__ unsigned pk(float a, float b) { f32x2 v = {a, b}; return __builtin_bit_cast(unsigned, __builtin_convertvector(v, bf16x2)); }
__device__ __forceinline__ bf16x8 asbf(u32x4 v) { return __builtin_bit_cast(bf16x8, v); }
__device__ __forceinline__ float bflo(unsigned v) { return U2F(v << 16); }
__device__ __forceinline__ float bfhi(unsigned v) { return U2F(v & 0xffff0000u); }
__device__ __forceinline__ bf16x8 lds128(lptr p) { return *(LAS bf16x8*)p; }
__device__ __forceinline__ bf16x8 ldstr(lptr p0, lptr p1) {
    u32x2 a = __builtin_bit_cast(u32x2, __builtin_amdgcn_ds_read_tr16_b64_v4i16((LAS s16x4*)p0));
    u32x2 b = __builtin_bit_cast(u32x2, __builtin_amdgcn_ds_read_tr16_b64_v4i16((LAS s16x4*)p1));
    return asbf((u32x4){a[0], a[1], b[0], b[1]}); }
typedef __amdgpu_buffer_rsrc_t rsrc_t;
__device__ __forceinline__ void st128(rsrc_t rs, unsigned off, u32x4 v) { __builtin_amdgcn_raw_buffer_store_b128(v, rs, off, 0, 16); }
__device__ __forceinline__ void st64(rsrc_t rs, unsigned off, u32x2 v) { __builtin_amdgcn_raw_buffer_store_b64(v, rs, off, 0, 16); }
__device__ __forceinline__ void st32(rsrc_t rs, unsigned off, unsigned v) { __builtin_amdgcn_raw_buffer_store_b32(v, rs, off, 0, 16); }
__device__ __forceinline__ u32x4 ld128(rsrc_t rs, unsigned off, unsigned soff) { return __builtin_amdgcn_raw_buffer_load_b128(rs, off, soff, 16); }
__device__ __forceinline__ unsigned ld32(rsrc_t rs, unsigned off) { return __builtin_amdgcn_raw_buffer_load_b32(rs, off, 0, 16); }
#define DMA_PRE "s_mov_b32 m0, %2\n\ts_nop 0\n\t"
#define DMA_OPS :: "v"(voff), "s"(sbase), "s"(ldst) : "memory"
__device__ __forceinline__ void dma1(const void* sbase, unsigned voff, unsigned ldst) { asm volatile(DMA_PRE "global_load_lds_dwordx4 %0, %1" DMA_OPS); }
__device__ __forceinline__ void dma1s(const void* sbase, unsigned voff, unsigned ldst) { asm volatile(DMA_PRE "global_load_lds_dwordx4 %0, %1 sc1" DMA_OPS); }
__device__ __forceinline__ void dma2s(const void* sbase, unsigned voff, unsigned ldst) {
    asm volatile(DMA_PRE "global_load_lds_dwordx4 %0, %1 sc1\n\tglobal_load_lds_dwordx4 %0, %1 offset:1024 sc1" DMA_OPS); }
__device__ __forceinline__ void dma4s(const void* sbase, unsigned voff, unsigned ldst) {
    asm volatile(DMA_PRE "global_load_lds_dwordx4 %0, %1 sc1\n\tglobal_load_lds_dwordx4 %0, %1 offset:1024 sc1\n\t"
                 "global_load_lds_dwordx4 %0, %1 offset:2048 sc1\n\tglobal_load_lds_dwordx4 %0, %1 offset:3072 sc1" DMA_OPS); }
__device__ __forceinline__ void dma7s(const char* src, unsigned voff, unsigned d) { dma4s(src, voff, d); dma2s(src + 4096, voff, d + 4096); dma1s(src + 6144, voff, d + 6144); }
template <int OFF> __device__ __forceinline__ void dmao(const void* sbase, unsigned voff, unsigned ldst) {
    asm volatile(DMA_PRE "global_load_lds_dwordx4 %0, %1 offset:%3 sc1" :: "v"(voff), "s"(sbase), "s"(ldst), "i"(OFF) : "memory"); }
#define WAITBAR(N) asm volatile("s_waitcnt vmcnt(" #N ") lgkmcnt(0)\n\ts_barrier" ::: "memory")
#define VBAR(N) asm volatile("s_waitcnt vmcnt(" #N ")\n\ts_barrier" ::: "memory")
#define LBAR() asm volatile("s_waitcnt lgkmcnt(0)\n\ts_barrier" ::: "memory")
#define DRAIN() asm volatile("s_waitcnt vmcnt(0)" ::: "memory")
__device__ __forceinline__ void poll(const char* ws, unsigned off, u64 tag) {
#pragma nounroll
    for (int spin = 0; spin < (1 << 18); ++spin) {
        if (ALL(__hip_atomic_load((gu64*)(ws + off), RLX_AGENT) == tag)) break;
    }
    asm volatile("" ::: "memory");
}

#define CODE_KB 28
#define OD 20
#define PD 16
#define STAGGER (2 * ((idx + (idx >> 2)) & 3))
constexpr int P1_SLOT = 24576;
constexpr int L_WQ = 5 * P1_SLOT;
constexpr int L_F = 32768;
constexpr int L_SJ = 98304;
constexpr int L_RING = 0;
constexpr int L_EX = 131072;
constexpr int L_M = L_WQ + 28672;
constexpr int L_XY = L_M + 512;
constexpr int L_Q = L_XY;
constexpr int L_FLG = L_XY + 8192;
constexpr int L_EPO = L_FLG + 2048;
constexpr int L_TAG = L_EPO + 1024;
constexpr int L_END = L_TAG + 48;
constexpr int L_Y = L_RING + 65536 + 16384;
constexpr int L_PART = L_EX;
#define L_OQ(q) ((q) < 2 ? 49152 + (q) * 7168 : ((q) - 2) * 7168)
constexpr int L_A = L_RING + 65536;
static_assert(L_PART + 16384 <= L_M && L_END <= 163840, "lds");
__device__ __forceinline__ void dma2v(const void* sbase, unsigned v0, unsigned v1, unsigned ldst) {
    asm volatile("s_mov_b32 m0, %3\n\ts_nop 0\n\tglobal_load_lds_dwordx4 %0, %2\n\tglobal_load_lds_dwordx4 %1, %2 offset:1024" :: "v"(v0), "v"(v1), "s"(sbase), "s"(ldst) : "memory"); }
__device__ __forceinline__ void dma4v(const void* sbase, unsigned v0, unsigned v1, unsigned v2, unsigned v3, unsigned ldst) {
    asm volatile("s_mov_b32 m0, %5\n\ts_nop 0\n\tglobal_load_lds_dwordx4 %0, %4\n\tglobal_load_lds_dwordx4 %1, %4 offset:1024\n\t"
                 "global_load_lds_dwordx4 %2, %4 offset:2048\n\tglobal_load_lds_dwordx4 %3, %4 offset:3072" :: "v"(v0), "v"(v1), "v"(v2), "v"(v3), "s"(sbase), "s"(ldst) : "memory"); }
__device__ __forceinline__ void dmap(const void* vptr, unsigned ldst) {
    asm volatile("s_mov_b32 m0, %1\n\ts_nop 0\n\tglobal_load_lds_dwordx4 %0, off" :: "v"(vptr), "s"(ldst) : "memory"); }
#define BAR() asm volatile("s_barrier" ::: "memory")
#define VBARN(N) asm volatile("s_waitcnt vmcnt(%0)\n\ts_barrier" :: "n"(N) : "memory")
#define NQ 4
#define CN(i) (6 + ((i) < NQ))
__device__ __forceinline__ void lds_arrive(lptr w, int lane) { if (lane == 0) __hip_atomic_fetch_add((LAS unsigned*)w, 1u, __ATOMIC_RELAXED, __HIP_MEMORY_SCOPE_WORKGROUP); }
__device__ __forceinline__ void lds_await(lptr w, unsigned n) {
#pragma nounroll
    for (int spin = 0; spin < (1 << 24) && (unsigned)__builtin_amdgcn_readfirstlane(*(volatile LAS unsigned*)w) < n; ++spin) __builtin_amdgcn_s_sleep(1);
    asm volatile("" ::: "memory");
}

template <bool SWAP>
__device__ __forceinline__ void p1_kv(lptr (&xr)[4], lptr (&wp)[2], bf16x8 (&xf)[28], rsrc_t rs, unsigned dst, int lane) {
    bf16x8 f[3][2];
    f32x16 acc[2] = {};
#define KOFF(k) ((((k) >> 2) % 5 % 2) * P1_SLOT)
#define KLX(k) xf[k] = lds128(xr[(k) & 3] + KOFF(k))
#define KLW(k, i) f[(k) % 3][i] = ldstr(wp[i] + KOFF(k) + ((k) & 3) * 2048, wp[i] + KOFF(k) + ((k) & 3) * 2048 + 512)
#define KMM(k, i) acc[i] = SWAP ? MFMA32(f[(k) % 3][i], xf[k], acc[i]) : MFMA32(xf[k], f[(k) % 3][i], acc[i])
    VBAR(2);
    KLX(0); KLW(0, 0); KLW(0, 1); SB(); KLX(1); KLW(1, 0); KLW(1, 1); SB();
#pragma unroll
    for (int k = 0; k < 28; ++k) {
        const int n = k + 2;
        if ((n & 3) == 0 && n < 28) {
            BAR();
            const int d = n == 8 || n == 16 ? 2 * P1_SLOT : n == 20 ? -4 * P1_SLOT : 0;
            if (d) { xr[0] += d; xr[1] += d; xr[2] += d; xr[3] += d; wp[0] += d; wp[1] += d; PIN(xr[0]); PIN(xr[1]); PIN(xr[2]); PIN(xr[3]); PIN(wp[0]); PIN(wp[1]); }
        }
        KMM(k, 0); SB(); if (n < 28) { KLX(n < 28 ? n : 0); KLW(n < 28 ? n : 0, 0); } SB();
        KMM(k, 1); SB(); if (n < 28) KLW(n < 28 ? n : 0, 1); SB();
    }
    const bool pad = SWAP ? lane >= 32 : (lane & 31) >= 28;
    const unsigned cv = !SWAP && (lane & 31) == 28 ? 0x3f803f80u : 0u;
#pragma unroll
    for (int i = 0; i < 2; ++i) {
        const f32x16 a = acc[i];
        u32x4 p0 = {pk(a[0], a[1]), pk(a[2], a[3]), pk(a[4], a[5]), pk(a[6], a[7])}, p1 = {pk(a[8], a[9]), pk(a[10], a[11]), pk(a[12], a[13]), pk(a[14], a[15])};
        if (!SWAP && pad) { p0 = (u32x4){cv, cv, cv, cv}; p1[0] = cv; p1[1] = cv; }
        if (pad) { p1[2] = cv; p1[3] = cv; }
        st128(rs, dst + i * (64 * 4096), p0); st128(rs, dst + i * (64 * 4096) + 1024, p1);
    }
    VBAR(4);
#undef KOFF
#undef KLX
#undef KLW
#undef KMM
}

__device__ __forceinline__ float rowmax16(const f32x16& s) {
    float m = __builtin_fmaxf(__builtin_fmaxf(s[0], s[1]), s[2]);
#pragma unroll
    for (int i = 3; i < 15; i += 2) m = __builtin_fmaxf(__builtin_fmaxf(m, s[i]), s[i + 1]);
    m = __builtin_fmaxf(m, s[15]);
    auto rr = __builtin_amdgcn_permlane32_swap(F2U(m), F2U(m), false, false);
    return __builtin_fmaxf(U2F(rr[0]), U2F(rr[1]));
}
__device__ __forceinline__ bf16x8 negbf(bf16x8 v) { u32x4 u = __builtin_bit_cast(u32x4, v); u ^= 0x80008000u; return asbf(u); }
__device__ __forceinline__ bf16x8 pk8(const f32x4& a, const f32x4& b) { return asbf((u32x4){pk(a[0], a[1]), pk(a[2], a[3]), pk(b[0], b[1]), pk(b[2], b[3])}); }

#define EX4(S, i) S[i] = EX(S[i]); S[i + 1] = EX(S[i + 1]); S[i + 2] = EX(S[i + 2]); S[i + 3] = EX(S[i + 3]);
#define CV2(P, j, S, i) P[j] = pk(S[i], S[i + 1]); P[j + 1] = pk(S[i + 2], S[i + 3]); PIN(P);
#define STEP(SC, SN, VP, KP, VNP, H0, H1, H2) do { \
        SN = MFMA32(k0, q0, cinit); SB(); v1 = lds128((VP) + 3072); H0; SB(); EX4(SC, 0) CV2(p0, 0, SC, 0) SB(); \
        o = MFMA32(v1p, asbf(p1), o); SB(); k0 = lds128(KP); H1; SB(); EX4(SC, 4) CV2(p0, 2, SC, 4) SB(); \
        SN = MFMA32(k1, q1, SN); SB(); H2; SB(); EX4(SC, 8) CV2(p1, 0, SC, 8) SB(); \
        o = MFMA32(v0, asbf(p0), o); SB(); k1 = lds128((KP) + 1024); SB(); v0 = lds128((VNP) + 2048); SB(); EX4(SC, 12) CV2(p1, 2, SC, 12) v1p = v1; SB(); \
        } while (0)
#define PAIRH(A0, A1, A2, B0, B1, B2) do { const int nxt = (cur + 8192) & 65535; lptr cp = ring + cur, np = ring + nxt; \
        STEP(sa, sb, cp, np, cp + 4096, A0, A1, A2); STEP(sb, sa, cp + 4096, np + 4096, np, B0, B1, B2); cur = nxt; } while (0)
#define NOP (void)0
#define DH(i) dmao<(i) * 1024>(dsrc, lv, ddst)
#define QUAD() do { PAIRH(NOP, NOP, DH(0), NOP, NOP, DH(1)); PAIRH(NOP, NOP, DH(2), NOP, NOP, DH(3)); } while (0)
#define ATT_INIT() PIN(cinit); f32x16 o = {}, sb; SB(); k0 = lds128(ring + 4096); SB(); k1 = lds128(ring + 5120); SB(); \
        bf16x8 v0 = lds128(ring + 2048), v1p = asbf((u32x4){0, 0, 0, 0}), v1; SB(); u32x4 p0, p1 = {0, 0, 0, 0}; int cur = 0;
#define KV_SRC(g, c) (kvh + (size_t)(28 * (g) + 4 * (c) + qt) * 4096)
#define KV_DST(g, c) (lds0 + L_RING + (g) * 65536 + ((c) & 3) * 16384 + qt * 4096)
#define KV_ISSUE(g, c) dma4s(KV_SRC(g, c), lv, KV_DST(g, c))

__device__ __forceinline__ void stage256(const char* src, unsigned lds0, int lane) {
    const unsigned v = (lane >> 1) * 128 + (lane & 1) * 16;
    dma1s(src, v, lds0 + L_FLG); dma1s(src + 4096, v, lds0 + L_FLG + 1024);
}
__device__ __forceinline__ void check256(const char* src, lptr lds, unsigned lds0, int lane, u64 tag) {
    const unsigned tl = (unsigned)tag, th = (unsigned)(tag >> 32);
#pragma nounroll
    for (int spin = 0; spin < (1 << 18); ++spin) {
        const u32x4 y0 = *(LAS u32x4*)(lds + L_FLG + lane * 16), y1 = *(LAS u32x4*)(lds + L_FLG + 1024 + lane * 16);
        if (ALL(y0[0] == tl && y0[1] == th && y0[2] == tl && y0[3] == th && y1[0] == tl && y1[1] == th && y1[2] == tl && y1[3] == th)) break;
        __builtin_amdgcn_s_sleep(4);
        stage256(src, lds0, lane); DRAIN();
    }
}

__global__ __launch_bounds__(512, 2) void fused_kernel(const u16* __restrict__ x, const u16* __restrict__ wqkv, const u16* __restrict__ gate_w, const u16* __restrict__ w_r,
                                                        const u16* __restrict__ w_i, const u16* __restrict__ wo, char* ws, u16* __restrict__ out) {
    __shared__ __attribute__((aligned(16))) char smem[L_END];
    lptr lds = (lptr)smem;
    const unsigned lds0 = (unsigned)(uintptr_t)lds;
    const int tid = __builtin_amdgcn_workitem_id_x(), lane = tid & 63, wid = __builtin_amdgcn_readfirstlane(tid >> 6);
    const int bid = __builtin_amdgcn_workgroup_id_x(), g8 = bid & 7, idx = bid >> 3;
    const int qt = wid & 3;
    typedef __attribute__((ext_vector_type(2))) u64 u64x2;
    u64x2 ka;
    asm volatile("s_load_dwordx4 %0, %1, 0" : "=s"(ka) : "s"(__builtin_amdgcn_kernarg_segment_ptr()));
    SB();
    const int rb = (g8 & 3) * 4 + (idx & 3), head = (g8 >> 2) * 8 + (idx >> 2), ut = rb * 4 + qt;
    const unsigned xo = (2 * rb + (qt >> 1)) * 896 + (qt & 1) * (32 * 28672), wo0 = 896 + head * 112 + 16 * qt * 2688;
    unsigned xv[4], wv[2];
#pragma unroll
    for (int j = 0; j < 4; ++j) xv[j] = (8 * j + (lane >> 3)) * 28672 + (((lane & 7) ^ ((lane >> 4) + 4 * (j & 1))) << 4) - j * 1024;
    wv[0] = (4 * (lane >> 5) + ((lane >> 2) & 3)) * 2688 + ((lane >> 4) & 1) * 64 + ((lane & 19) == 19 ? 2 : lane & 3) * 16;
    wv[1] = wv[0] + 8 * 2688 - 1024;
    const int c0 = STAGGER;
#define CHUNK(i) ((unsigned)(0x65432106543210ull >> (4 * (c0 + (i)))) & 7u)
#define P1_ISSUE(i, slot) do { const unsigned cc = CHUNK(i), d = lds0 + (slot) * P1_SLOT; dma4v(xb + cc * 128, xv[0], xv[1], xv[2], xv[3], d + qt * 4096); \
        dma2v(wb + cc * (64 * 2688), wv[0], wv[1], d + 16384 + qt * 2048); } while (0)
    {
        const unsigned cc = CHUNK(wid >> 2);
        unsigned a = xo + cc * 128, b = wo0 + cc * (64 * 2688), d = lds0 + (wid >> 2) * P1_SLOT + qt * 4096, e = lds0 + (wid >> 2) * P1_SLOT + 16384 + qt * 2048;
        asm volatile("s_waitcnt lgkmcnt(0)" : "+s"(ka), "+s"(a), "+s"(b), "+s"(d), "+s"(e), "+v"(xv[0]), "+v"(xv[1]), "+v"(xv[2]), "+v"(xv[3]), "+v"(wv[0]), "+v"(wv[1]));
        SB();
        dma4v((const char*)ka[0] + a, xv[0], xv[1], xv[2], xv[3], d); dma2v((const char*)ka[1] + b, wv[0], wv[1], e);
    }
    SB();
    const char* xb = (const char*)ka[0] + xo; const char* wb = (const char*)ka[1] + wo0;
    const char* qb = (const char*)ka[1] + head * 56 + 16 * qt * 2688;
    const unsigned qv = (lane >> 2) * 2688 + ((lane & 3) == 3 ? 40 : (lane & 3) * 16);
#define WQ_ISSUE(i) dma1(qb + CHUNK(i) * (64 * 2688), qv, lds0 + L_WQ + (i) * 4096 + qt * 1024)
    if (NQ >= 2) WQ_ISSUE(wid >> 2);
    const rsrc_t rs = __builtin_amdgcn_make_buffer_rsrc(ws, 0, WS_END, 0x00020000);
    const unsigned lv = lane * 16;
    const int r = lane & 31, h = lane >> 5;
    const int ut3 = rb * 4 + (head >> 2), win = (head & 3) * 4 + qt;
    const char* kvh = ws + WS_KV + (size_t)head * 64 * 4096;
    lptr part = lds + L_PART + qt * 4096 + lv;
    lptr xch = lds + L_A + qt * 4096 + lv;
    if (wid >= 4) {
        const char* fs0 = (const char*)gate_w + qt * 32; const char* fs1 = (const char*)&c_F + head * 28672 + qt * 1024;
        asm volatile("" : "+s"(fs0), "+s"(fs1));
        const char* fb = (r == 28 ? fs0 : fs1) + (r == 28 ? h * 16 : lv);
        const int fsh = r == 28 ? 7 : 12;
        const int row0 = 2 * (bid & 31) + 256 * (bid >> 5);
        const int wsh = qt == 3 ? 26 : 2;
        const unsigned vw = ((lane >> 3) & 3) * (64 * 450) + h * (450 - wsh) + (lane & 7) * 16;
        asm volatile("" : "+v"(fb));
        *(LAS u32x4*)(lds + L_TAG) = (u32x4){0u, 0u, 0u, 0u}; *(LAS u32x4*)(lds + L_TAG + 16) = (u32x4){0u, 0u, 0u, 0u}; *(LAS u32x4*)(lds + L_TAG + 32) = (u32x4){0u, 0u, 0u, 0u};
        P1_ISSUE(2, 2); if (NQ > 2) WQ_ISSUE(2);
        LBAR();
        P1_ISSUE(3, 3); if (NQ > 3) WQ_ISSUE(3);
        VBARN(CN(2) + CN(3));
        P1_ISSUE(4, 4); if (NQ > 4) WQ_ISSUE(4);
        VBARN(CN(3) + CN(4));
        P1_ISSUE(5, 0); if (NQ > 5) WQ_ISSUE(5);
        VBARN(CN(4) + CN(5));
        P1_ISSUE(6, 1); if (NQ > 6) WQ_ISSUE(6);
        VBARN(CN(5) + CN(6));
        VBARN(CN(6));
        VBAR(0);
        BAR();
#pragma unroll
        for (int i = NQ; i < 7; ++i) WQ_ISSUE(i);
        VBAR(3);
#pragma unroll
        for (int i = 0; i < 7; ++i) { unsigned cc = CHUNK(i), d = lds0 + L_F + i * 4096 + qt * 1024; asm volatile("" : "+s"(cc), "+s"(d)); dmap(fb + (cc << fsh), d); if (i == 2) VBAR(3); }
        dma1(w_r + row0 * 225 + 58 * qt, vw, lds0 + L_SJ + qt * 2048); dma1(w_i + row0 * 225 + 58 * qt, vw, lds0 + L_SJ + qt * 2048 + 1024);
        const u64 tag = *(LAS u64*)(lds + L_EPO) + 1;
        if (wid == 4) {
            __builtin_amdgcn_s_sleep(PD);
            poll(ws, WS_FA + ((g8 & 3) * 256 + (((lane >> 4) & 1) * 8 + (head >> 1)) * 16 + (lane & 15)) * 128, tag);
        }
        VBAR(0);
        lptr ring = lds + L_RING + 65536 + lv;
        KV_ISSUE(1, 0); KV_ISSUE(1, 1); KV_ISSUE(0, 0);
        const bf16x8 q0 = lds128(lds + L_Q + qt * 2048 + lv), q1 = lds128(lds + L_Q + qt * 2048 + 1024 + lv);
        {
            lptr sp = lds + L_SJ + qt * 2048;
            const int h2 = lane & 1, fl = lane < 57 - 7 * (qt == 3) ? lane : 57 - 7 * (qt == 3);
            unsigned wr[8], wi[8];
#pragma unroll
            for (int k = 0; k < 8; ++k) { lptr b = sp + k * 128 + fl * 2 + (k >> 2) * wsh; wr[k] = *(LAS u16*)b; wi[k] = *(LAS u16*)(b + 1024); }
            u32x4 ev, od;
#pragma unroll
            for (int k = 0; k < 4; ++k) {
                const unsigned A = wr[k] | (wi[k] << 16), B = wr[4 + k] | (wi[4 + k] << 16);
                const unsigned got = __builtin_amdgcn_mov_dpp(h2 ? A : B, 0xB1, 0xF, 0xF, true);
                ev[k] = h2 ? got : A; od[k] = h2 ? B : got;
            }
            st128(rs, WS_WT + bid * 8192 + qt * 1024 + lv, ev); st128(rs, WS_WT + bid * 8192 + 4096 + qt * 1024 + lv, od);
        }
        WAITBAR(10);
        bf16x8 k0 = lds128(ring), k1 = lds128(ring + 1024);
        f32x16 cinit, sa = {};
        sa = MFMA32(k0, q0, sa); sa = MFMA32(k1, q1, sa);
        const float m = rowmax16(sa);
        if (lane < 32) *(LAS float*)(lds + L_M + qt * 128 + lane * 4) = m;
#pragma unroll
        for (int i = 0; i < 16; ++i) { sa[i] -= m; cinit[i] = -m; }
        ATT_INIT()
        {
            const char* dsrc = KV_SRC(1, 2); const unsigned ddst = KV_DST(1, 2);
            PAIRH(NOP, DH(0), DH(1), NOP, DH(2), DH(3));
        }
#pragma nounroll
        for (int j = 0; j < 7; ++j) {
            VBAR(4);
            const char* dsrc = j < 6 ? KV_SRC(1, j + 3) : (const char*)&c_G + idx * 14336 + (qt < 3 ? qt * 4096 : 10240);
            const unsigned ddst = j < 6 ? KV_DST(1, j + 3) : lds0 + L_EX + 16384;
            QUAD();
        }
        __builtin_amdgcn_s_setprio(1);
        VBAR(4);
        bf16x8 wf[28];
        gcptr wsrc = (gcptr)(uintptr_t)c_G.t[0].v + (size_t)win * 28672 + lv;
#define GLD(ks) wf[ks] = *(const GAS bf16x8*)(wsrc + (ks) * 1024)
        {
            const char* dsrc = ws + WS_YT + (size_t)ut3 * 28672 + qt * 7168; const unsigned ddst = lds0 + L_Y + qt * 7168;
            PAIRH(DH(0), DH(1), DH(2), DH(3), dmao<0>(dsrc + 4096, lv, ddst + 4096), dmao<1024>(dsrc + 4096, lv, ddst + 4096));
            PAIRH(dmao<2048>(dsrc + 4096, lv, ddst + 4096), GLD(0), GLD(1), GLD(2), GLD(3), GLD(4));
            PAIRH(GLD(5), GLD(6), GLD(7), GLD(8), GLD(9), GLD(10));
        }
        o = MFMA32(v1p, asbf(p1), o);
#pragma unroll
        for (int i = 0; i < 4; ++i) *(LAS f32x4*)(part + i * 1024) = (f32x4){o[4 * i], o[4 * i + 1], o[4 * i + 2], o[4 * i + 3]};
        asm volatile("" ::: "memory");
        *(volatile LAS unsigned*)(lds + L_TAG + 32 + qt * 4) = 1u;
        __builtin_amdgcn_s_setprio(0);
        asm volatile("s_waitcnt vmcnt(11)" ::: "memory");
        lds_arrive(lds + L_TAG + 12, lane);
#pragma unroll
        for (int ks = 11; ks < 28; ++ks) { GLD(ks); SB(); }
        lds_await(lds + L_TAG + 12, 4);
        f32x16 acc = {};
        lptr y = lds + L_Y + (8 * h + ((lane & 15) >> 2)) * 64 + ((lane >> 4) & 1) * 32 + (lane & 3) * 8;
        PIN(y);
#pragma unroll
        for (int ks = 0; ks < 28; ++ks) acc = MFMA32(wf[ks], ldstr(y + ks * 1024, y + ks * 1024 + 256), acc);
#pragma unroll
        for (int i = 0; i < 4; ++i) *(LAS f32x4*)(xch + i * 1024) = (f32x4){acc[4 * i], acc[4 * i + 1], acc[4 * i + 2], acc[4 * i + 3]};
        LBAR();
        return;
    }
    const int gw = bid * 4 + qt;
    {
        const char* pc_; asm volatile("s_getpc_b64 %0" : "=s"(pc_));
        dma1s(wid == 0 ? pc_ + (idx < CODE_KB ? idx : 0) * 1024 : wid == 1 ? (const char*)&c_T + idx * 1024 : wid == 2 ? (const char*)&c_T + 32768 + (idx & 3) * 1024 : ws + WS_EP,
              wid == 3 ? 0u : lv, lds0 + (wid == 3 ? L_EPO : L_XY));
        const int hf = gw < 896 ? gw : 895, wn = hf / 56, sl = hf - wn * 56, hp = sl / 7, pc = sl - hp * 7;
        const int ka = pc < 6 ? 28 * (2 * hp + (pc >= 3)) + (pc % 3 == 2 ? 16 : (pc % 3) * 4) : 56 * hp + 20, kd = pc < 6 ? 8 : 28;
        const u16* wop = wo + ka * 448 + 28 * wn;
        asm volatile("s_nop 4" : "+s"(wop));
        if (lane < 32) dma1(wop, (((lane >> 4) & 1) * kd + ((lane >> 2) & 3)) * 896 + ((lane & 3) == 3 ? 40 : (lane & 3) * 16), lds0 + L_FLG + qt * 512);
    }
    bf16x8 q0, q1;
    u64 tag;
    u32x4 wof;
    {
        bf16x8 xf[28];
        const int blk = (lane >> 4) & 1, q4 = (lane & 15) >> 2, p4 = lane & 3;
        {
            lptr xr[4], wp[2];
#pragma unroll
            for (int ks = 0; ks < 4; ++ks) { xr[ks] = lds + (32 * qt + r) * 128 + (((2 * ks + h) ^ ((r >> 1) & 7)) << 4); PIN(xr[ks]); }
#pragma unroll
            for (int i = 0; i < 2; ++i) { const int o = 56 * i + 32 * blk + 8 * p4; wp[i] = lds + 16384 + h * 1024 + (o >> 6) * 256 + q4 * 64 + (o & 63); PIN(wp[i]); }
            unsigned dst = WS_KV + (2 * (head & 7) * 64 + ut) * 4096 + (head >> 3) * 2048 + lv;
            PIN(dst);
            if (head < 8) p1_kv<true>(xr, wp, xf, rs, dst, lane); else p1_kv<false>(xr, wp, xf, rs, dst, lane);
        }
        tag = *(LAS u64*)(lds + L_EPO) + 1;
        {
            unsigned e[8];
#pragma unroll
            for (int k = 0; k < 8; ++k) e[k] = *(LAS u16*)(lds + L_FLG + qt * 512 + k * 64 + (r < 24 ? r * 2 : r < 28 ? r * 2 + 8 : 0));
            const unsigned em = r < 28 ? ~0u : 0u;
            wof = (u32x4){(e[0] | (e[1] << 16)) & em, (e[2] | (e[3] << 16)) & em, (e[4] | (e[5] << 16)) & em, (e[6] | (e[7] << 16)) & em};
            PIN(wof);
        }
#define PUBLISH_KV() do { DRAIN(); unsigned old = 0; \
            if (lane == 0) old = __hip_atomic_fetch_add((LAS unsigned*)(lds + L_TAG), 1u, __ATOMIC_RELAXED, __HIP_MEMORY_SCOPE_WORKGROUP); \
            if (__builtin_amdgcn_readfirstlane(old) == 3 && lane < 32) \
                st128(rs, WS_FA + ((lane >> 3) * 256 + head * 16 + rb) * 128 + (lane & 7) * 16, (u32x4){(unsigned)tag, (unsigned)(tag >> 32), 0u, 0u}); } while (0)
        if (NQ < 4) PUBLISH_KV();
        BAR();
        {
            const int o = 32 * blk + 8 * p4;
            lptr wq = lds + L_WQ + h * 512 + q4 * 64 + (o < 48 ? o : o == 48 ? 56 : 48);
            PIN(wq);
            bf16x8 f[4];
            f32x16 a = {};
#pragma unroll
            for (int k = 0; k < 3; ++k) { f[k] = ldstr(wq + k * 1024, wq + k * 1024 + 256); SB(); }
#pragma unroll
            for (int k = 0; k < 28; ++k) { if (k == 13) { if (NQ >= 4) PUBLISH_KV(); BAR(); } if (k < 25) f[(k + 3) & 3] = ldstr(wq + (k + 3) * 1024, wq + (k + 3) * 1024 + 256); SB(); a = MFMA32(f[k & 3], xf[k], a); SB(); }
            a *= 0.18898223650461362f * 1.4426950408889634f;
            q0 = asbf((u32x4){pk(a[0], a[1]), pk(a[2], a[3]), pk(a[4], a[5]), pk(a[6], a[7])});
            q1 = asbf((u32x4){pk(a[8], a[9]), pk(a[10], a[11]), h ? 0u : pk(a[12], a[13]), h ? 0u : pk(a[14], a[15])});
            *(LAS bf16x8*)(lds + L_Q + qt * 2048 + lv) = q0; *(LAS bf16x8*)(lds + L_Q + qt * 2048 + 1024 + lv) = q1;
        }
        LBAR();
        f32x16 af = {};
        {
            lptr fr = lds + L_F + lv;
            PIN(fr);
            bf16x8 f[4];
#pragma unroll
            for (int k = 0; k < 3; ++k) { f[k] = lds128(fr + k * 1024); SB(); }
#pragma unroll
            for (int k = 0; k < 28; ++k) { if (k < 25) f[(k + 3) & 3] = lds128(fr + (k + 3) * 1024); SB(); af = MFMA32(xf[k], f[k & 3], af); SB(); }
        }
        BAR();
        KV_ISSUE(0, 1);
        {
            lptr sp = lds + L_XY + qt * 2048;
            if (r < 28) {
                const int sx = (r >> 1) & 3;
                *(LAS u32x4*)(sp + r * 64 + ((h ^ sx) << 4)) = (u32x4){pk(af[0], af[1]), pk(af[2], af[3]), pk(af[4], af[5]), pk(af[6], af[7])};
                *(LAS u32x4*)(sp + r * 64 + (((2 + h) ^ sx) << 4)) = (u32x4){pk(af[8], af[9]), pk(af[10], af[11]), pk(af[12], af[13]), pk(af[14], af[15])};
            } else if (r == 28) {
#pragma unroll
                for (int i = 0; i < 4; ++i) *(LAS f32x4*)(sp + 1792 + h * 64 + i * 16) = (f32x4){af[4 * i], af[4 * i + 1], af[4 * i + 2], af[4 * i + 3]};
            }
            const unsigned xd = WS_XT + ut * 28672 + head * 1792 + lv;
            const int sl = (lane & ~3) | ((lane ^ (lane >> 3)) & 3);
            st128(rs, xd, *(LAS u32x4*)(sp + sl * 16));
            st128(rs, lane < 48 ? xd + 1024 : lane < 56 && head == 0 ? WS_G + ut * 128 + (lane - 48) * 16 : 0x40000000u, *(LAS u32x4*)(sp + 1024 + (lane < 48 ? sl : lane) * 16));
        }
    }
    const unsigned odst = WS_OT + ut * 28672 + (head >> 1) * 3584 + r * 16;
    {
        const u32x4 sent = {~0u, ~0u, ~0u, ~0u};
        st128(rs, odst + ((head & 1) * 3 + h) * 512, sent);
        st128(rs, h == 0 ? odst + ((head & 1) * 3 + 2) * 512 : (head & 1) ? 0x40000000u : odst + 6 * 512, sent);
        st128(rs, gw < 896 && !h ? WS_WO + gw * 512 + lv : 0x40000000u, wof);
    }
    BAR();
    lptr ring = lds + L_RING + lv;
    bf16x8 k0 = lds128(ring), k1 = lds128(ring + 1024);
    f32x16 cinit, sa;
    {
        const float m = *(LAS float*)(lds + L_M + qt * 128 + r * 4);
#pragma unroll
        for (int i = 0; i < 16; ++i) cinit[i] = -m;
    }
    sa = MFMA32(k0, q0, cinit); sa = MFMA32(k1, q1, sa);
    ATT_INIT()
    {
        const char* dsrc = KV_SRC(0, 2); const unsigned ddst = KV_DST(0, 2);
        PAIRH(NOP, DH(0), DH(1), NOP, DH(2), DH(3));
    }
    {
        asm volatile("s_waitcnt vmcnt(4)" ::: "memory");
        if (lane == 0 && __hip_atomic_fetch_add((LAS unsigned*)(lds + L_TAG + 4), 1u, __ATOMIC_RELAXED, __HIP_MEMORY_SCOPE_WORKGROUP) == 3)
            __hip_atomic_store((gu64*)(ws + WS_FX + (bid >> 2) * 128 + (bid & 3) * 8), tag, RLX_AGENT);
    }
    BAR();
    {
        const char* dsrc = KV_SRC(0, 3); const unsigned ddst = KV_DST(0, 3);
        QUAD();
    }
    VBAR(4);
    bf16x8 c64[2], s64[2], c32[2], s32[2]; f32x4 twc[2], tws[2];
#define TLD(i) do { c64[i] = *(const bf16x8*)c_T.t64[qt][0][i][lane]; s64[i] = *(const bf16x8*)c_T.t64[qt][1][i][lane]; } while (0)
#define TLD2(i) do { c32[i] = *(const bf16x8*)c_T.t32[0][i][lane]; s32[i] = *(const bf16x8*)c_T.t32[1][i][lane]; } while (0)
#define TLD3(i) do { twc[i] = *(const f32x4*)c_T.tw[qt][i][0][lane]; tws[i] = *(const f32x4*)c_T.tw[qt][i][1][lane]; } while (0)
    {
        const char* dsrc = KV_SRC(0, 4); const unsigned ddst = KV_DST(0, 4);
        PAIRH(TLD(0), TLD(1), TLD2(0), TLD2(1), TLD3(0), TLD3(1));
        PAIRH(if (wid == 0) stage256(ws + WS_FX, lds0, lane), DH(0), DH(1), DH(2), DH(3), NOP);
    }
    if (wid == 0) { asm volatile("s_waitcnt vmcnt(4)" ::: "memory"); check256(ws + WS_FX, lds, lds0, lane, tag); }
    VBAR(4);
    const int f = g8 * 32 + idx;
    const bool do_fft = f < 225, cplx = f != 0 && f != 224;
    const int c = lane & 15, g = lane >> 4;
    u32x4 wq[2];
    {
        const int fq = f < 232 ? f / 58 : 0, fl = f < 232 ? f - 58 * fq : 0;
        const unsigned wo_ = (g * 32 + (c >> 1)) * 8192 + (c & 1) * 16, wso = WS_WT + qt * 8 * 8192 + (fl & 1) * 4096 + fq * 1024 + (fl >> 1) * 32;
        const char* xs = ws + WS_XT + (f < 224 ? f : 0) * 128 + qt * (16 * 28672);
        const int pa = c ^ ((8 * qt + g) & 15), pb = pa ^ 4;
        PAIRH(dma1s(xs, (2 * g + (pa >> 3)) * 28672 + (pa & 7) * 16, lds0 + L_XY + qt * 2048), dma1s(xs + 8 * 28672, (2 * g + (pb >> 3)) * 28672 + (pb & 7) * 16, lds0 + L_XY + qt * 2048 + 1024),
              wq[0] = ld128(rs, wo_, wso), wq[1] = ld128(rs, wo_, wso + 128 * 8192), NOP, NOP);
    }
    asm volatile("s_waitcnt vmcnt(2)" ::: "memory");
    lds_arrive(lds + L_TAG + 8, lane);
    lds_await(lds + L_TAG + 8, 4);
    const f32x4 z = {};
    bf16x8 n64[2];
    if (do_fft) {
        bf16x8 ar[2][2], ai[2][2], n32[2];
        const int pre = f == 224 ? 4 : 0;
#pragma unroll
        for (int t = 0; t < 2; ++t)
#pragma unroll
            for (int ks = 0; ks < 2; ++ks) {
                lptr a = lds + L_XY + (16 * t + c) * 256;
                ar[t][ks] = lds128(a + (((8 * ks + pre + g) ^ c) << 4)); ai[t][ks] = lds128(a + (((8 * ks + 4 + g) ^ c) << 4));
            }
        if (!cplx) { ai[0][0] = ai[0][1] = ai[1][0] = ai[1][1] = asbf((u32x4){0, 0, 0, 0}); }
#pragma unroll
        for (int i = 0; i < 2; ++i) { n64[i] = negbf(s64[i]); n32[i] = negbf(s32[i]); }
        f32x4 dr[2], di[2], er[2], ei[2];
#pragma unroll
        for (int t = 0; t < 2; ++t) {
            dr[t] = MFMA16(ar[t][0], c64[0], z); dr[t] = MFMA16(ar[t][1], c64[1], dr[t]); dr[t] = MFMA16(ai[t][0], s64[0], dr[t]); dr[t] = MFMA16(ai[t][1], s64[1], dr[t]);
            di[t] = MFMA16(ai[t][0], c64[0], z); di[t] = MFMA16(ai[t][1], c64[1], di[t]); di[t] = MFMA16(ar[t][0], n64[0], di[t]); di[t] = MFMA16(ar[t][1], n64[1], di[t]);
        }
#pragma unroll
        for (int t = 0; t < 2; ++t) { er[t] = dr[t] * twc[t] + di[t] * tws[t]; ei[t] = di[t] * twc[t] - dr[t] * tws[t]; }
        bf16x8 br = pk8(er[0], er[1]), bi = pk8(ei[0], ei[1]);
#pragma unroll
        for (int t = 0; t < 2; ++t) {
            dr[t] = MFMA16(c32[t], br, z); dr[t] = MFMA16(s32[t], bi, dr[t]);
            di[t] = MFMA16(c32[t], bi, z); di[t] = MFMA16(n32[t], br, di[t]);
        }
#pragma unroll
        for (int t = 0; t < 2; ++t) {
            const f32x4 wr = {bflo(wq[t][0]), bflo(wq[t][1]), bflo(wq[t][2]), bflo(wq[t][3])}, wi = {bfhi(wq[t][0]), bfhi(wq[t][1]), bfhi(wq[t][2]), bfhi(wq[t][3])};
            er[t] = dr[t] * wr - di[t] * wi; ei[t] = dr[t] * wi + di[t] * wr;
        }
        br = pk8(er[0], er[1]); bi = pk8(ei[0], ei[1]);
#pragma unroll
        for (int t = 0; t < 2; ++t) {
            dr[t] = MFMA16(c32[t], br, z); dr[t] = MFMA16(n32[t], bi, dr[t]);
            di[t] = MFMA16(c32[t], bi, z); di[t] = MFMA16(s32[t], br, di[t]);
        }
#pragma unroll
        for (int t = 0; t < 2; ++t) {
            er[t] = dr[t] * twc[t] - di[t] * tws[t]; ei[t] = di[t] * twc[t] + dr[t] * tws[t];
            lptr e = lds + L_EX + (16 * qt + c) * 72 + t * 32 + g * 8;
            *(LAS u32x2*)e = (u32x2){pk(er[t][0], er[t][1]), pk(er[t][2], er[t][3])};
            *(LAS u32x2*)(e + 4608) = (u32x2){pk(ei[t][0], ei[t][1]), pk(ei[t][2], ei[t][3])};
        }
    }
    __builtin_amdgcn_s_waitcnt(0x0F70);
    SB();
    {
        const char* dsrc = KV_SRC(0, 5); const unsigned ddst = KV_DST(0, 5);
        LBAR();
        if (do_fft) {
            lptr e = lds + L_EX + (16 * (g >> 1) + 4 * (g & 1) + (c >> 2)) * 72 + (c & 3) * 8;
            PIN(e);
#pragma unroll
            for (int t = 0; t < 2; ++t) {
                bf16x8 ur[2], ui[2];
#pragma unroll
                for (int ks = 0; ks < 2; ++ks) {
                    ur[ks] = ldstr(e + ks * 2304 + t * 32, e + ks * 2304 + t * 32 + 576);
                    ui[ks] = ldstr(e + 4608 + ks * 2304 + t * 32, e + 4608 + ks * 2304 + t * 32 + 576);
                }
                f32x4 yr = MFMA16(c64[0], ur[0], z); yr = MFMA16(c64[1], ur[1], yr); yr = MFMA16(n64[0], ui[0], yr); yr = MFMA16(n64[1], ui[1], yr);
                f32x4 yi = MFMA16(c64[0], ui[0], z); yi = MFMA16(c64[1], ui[1], yi); yi = MFMA16(s64[0], ur[0], yi); yi = MFMA16(s64[1], ur[1], yi);
                lptr d = lds + L_XY + ((16 * t + c) * 2 + (qt >> 1)) * 128 + (g & 1) * 8;
                const int q8 = (2 * (qt & 1) + (g >> 1)) ^ (c & 7);
                *(LAS u32x2*)(d + q8 * 16) = (u32x2){pk(yr[0], yr[1]), pk(yr[2], yr[3])};
                *(LAS u32x2*)(d + (q8 ^ 4) * 16) = (u32x2){pk(yi[0], yi[1]), pk(yi[2], yi[3])};
            }
        }
        lds_arrive(lds + L_TAG + 8, lane);
        lds_await(lds + L_TAG + 8, 8);
        {
            const int pr = f == 0 ? 0 : f == 224 ? 1 : 2 * f;
            const unsigned yd = !do_fft || (!cplx && (lane & 4)) ? 0x40000000u : WS_YT + (qt * 16 + (lane >> 3)) * 28672 + pr * 64 + (lane & 7) * 16;
#pragma unroll
            for (int i = 0; i < 2; ++i) { const int ln = qt * 16 + i * 8 + (lane >> 3);
                st128(rs, yd + i * (8 * 28672), *(LAS u32x4*)(lds + L_XY + ln * 128 + (((lane & 7) ^ ((ln >> 1) & 7)) << 4))); }
        }
        SB();
        PAIRH(NOP, DH(0), DH(1), NOP, DH(2), DH(3));
    }
    BAR();
    {
        const char* dsrc = KV_SRC(0, 6); const unsigned ddst = KV_DST(0, 6);
        PAIRH(NOP, NOP, NOP, NOP, NOP, NOP);
        asm volatile("s_waitcnt vmcnt(4)" ::: "memory");
        if (lane == 0 && __hip_atomic_fetch_add((LAS unsigned*)(lds + L_TAG + 4), 1u, __ATOMIC_RELAXED, __HIP_MEMORY_SCOPE_WORKGROUP) == 7)
            __hip_atomic_store((gu64*)(ws + WS_FY + (bid >> 2) * 128 + (bid & 3) * 8), tag, RLX_AGENT);
        PAIRH(NOP, DH(0), DH(1), NOP, DH(2), DH(3));
    }
    VBAR(4);
    const float gz = U2F(ld32(rs, WS_G + ut3 * 128 + ((r >> 2) & 1) * 64 + ((r & 3) + 4 * (r >> 3)) * 4));
    bf16x8 wf[28];
#define WLD(i) do { if ((i) < 28) wf[(i) < 28 ? (i) : 0] = asbf(ld128(rs, lv + ((i) % 7 & 3) * 1024, WS_WO + win * 28672 + ((qt + (i) / 7) & 3) * 7168 + ((i) % 7 >> 2) * 4096)); } while (0)
#pragma unroll
    for (int p = 0; p < 5; ++p) {
        if (p == 1 && wid == 0) stage256(ws + WS_FY, lds0, lane);
        if (p == 2) {
            if (wid == 0) {
                asm volatile("s_waitcnt vmcnt(6)" ::: "memory");
                check256(ws + WS_FY, lds, lds0, lane, tag);
                if (bid == 0 && lane == 0) __hip_atomic_store((gu64*)(ws + WS_EP), tag, RLX_AGENT);
            }
            VBAR(12);
        }
        PAIRH(WLD(6 * p), WLD(6 * p + 1), WLD(6 * p + 2), WLD(6 * p + 3), WLD(6 * p + 4), WLD(6 * p + 5));
    }
    o = MFMA32(v1p, asbf(p1), o);
    lds_await(lds + L_TAG + 32 + qt * 4, 1);
#pragma unroll
    for (int i = 0; i < 4; ++i) { const f32x4 t = *(LAS f32x4*)(part + i * 1024); o[4 * i] += t[0]; o[4 * i + 1] += t[1]; o[4 * i + 2] += t[2]; o[4 * i + 3] += t[3]; }
    float l = U2F(__builtin_amdgcn_permlane32_swap(F2U(o[12]), F2U(o[12]), false, false)[1]);
    if (__builtin_expect(ANY(!(l <= 1073741824.f)), 0)) {
        const unsigned kb = WS_KV + head * 64 * 4096;
        const f32x16 z16 = {};
        float m = -INFINITY;
#pragma nounroll
        for (int t = 0; t < 64; ++t) {
            f32x16 s = MFMA32(asbf(ld128(rs, lv, kb + t * 4096)), q0, z16); s = MFMA32(asbf(ld128(rs, lv + 1024, kb + t * 4096)), q1, s);
            m = __builtin_fmaxf(m, rowmax16(s));
        }
#pragma unroll
        for (int i = 0; i < 16; ++i) { cinit[i] = -m; o[i] = 0.f; }
#pragma nounroll
        for (int t = 0; t < 64; ++t) {
            f32x16 s = MFMA32(asbf(ld128(rs, lv, kb + t * 4096)), q0, cinit); s = MFMA32(asbf(ld128(rs, lv + 1024, kb + t * 4096)), q1, s);
            EX4(s, 0) EX4(s, 4) EX4(s, 8) EX4(s, 12) CV2(p0, 0, s, 0) CV2(p0, 2, s, 4) CV2(p1, 0, s, 8) CV2(p1, 2, s, 12)
            o = MFMA32(asbf(ld128(rs, lv + 2048, kb + t * 4096)), asbf(p0), o); o = MFMA32(asbf(ld128(rs, lv + 3072, kb + t * 4096)), asbf(p1), o);
        }
        l = U2F(__builtin_amdgcn_permlane32_swap(F2U(o[12]), F2U(o[12]), false, false)[1]);
    }
    {
        o *= __builtin_amdgcn_rcpf(l);
        u32x4 oa = {pk(o[0], o[1]), pk(o[2], o[3]), pk(o[4], o[5]), pk(o[6], o[7])}, ob = {pk(o[8], o[9]), pk(o[10], o[11]), pk(o[12], o[13]), pk(o[14], o[15])};
#pragma unroll
        for (int i = 0; i < 4; ++i) { oa[i] = UMIN(oa[i], 0xfffffffeu); ob[i] = UMIN(ob[i], 0xfffffffeu); }
        st128(rs, odst + ((head & 1) * 3 + h) * 512, oa);
        if (h == 0) st128(rs, odst + ((head & 1) * 3 + 2) * 512, ob); else st64(rs, odst + 6 * 512 + (head & 1) * 8, (u32x2){ob[0], ob[1]});
    }
    const float gate = __builtin_amdgcn_rcpf(1.f + EX(-1.4426950408889634f * gz));
    __builtin_amdgcn_s_sleep(OD);
    u32x4 of[7];
#pragma nounroll
    for (int spin = 0; spin < (1 << 18); ++spin) {
        dma7s(ws + WS_OT + (size_t)ut3 * 28672 + qt * 7168, lv, lds0 + L_OQ(qt));
        DRAIN();
        unsigned mx = 0;
#pragma unroll
        for (int i = 0; i < 7; ++i) { of[i] = *(LAS u32x4*)(lds + L_OQ(qt) + i * 1024 + lv); mx = UMAX(UMAX(mx, UMAX(of[i][0], of[i][1])), UMAX(of[i][2], of[i][3])); }
        if (ALL(mx != ~0u)) break;
    }
    __builtin_amdgcn_s_waitcnt(0x0F70);
    lds_arrive(lds + L_TAG + 16, lane);
    f32x16 acc = MFMA32(wf[0], asbf(of[0]), f32x16{}); SB();
    acc = MFMA32(wf[1], asbf(of[1]), acc); SB();
    acc = MFMA32(wf[2], asbf(of[2]), acc); SB();
    lds_await(lds + L_TAG + 16, 4);
    f32x4 av[4];
    {
        lptr ob[3];
#pragma unroll
        for (int i = 0; i < 3; ++i) ob[i] = lds + L_OQ((qt + 1 + i) & 3) + lv;
        of[0] = *(LAS u32x4*)ob[0]; of[1] = *(LAS u32x4*)(ob[0] + 1024); of[2] = *(LAS u32x4*)(ob[0] + 2048); SB();
#pragma unroll
        for (int i = 3; i < 28; ++i) {
            if (i == 22) { BAR(); av[0] = *(LAS f32x4*)xch * gate; av[1] = *(LAS f32x4*)(xch + 1024) * gate; av[2] = *(LAS f32x4*)(xch + 2048) * gate; av[3] = *(LAS f32x4*)(xch + 3072) * gate; }
            acc = MFMA32(wf[i], asbf(of[i % 7]), acc); SB(); if (i < 21) of[i % 7] = *(LAS u32x4*)(ob[i / 7] + (i % 7) * 1024); SB(); }
    }
    lptr stg = lds + L_A + qt * 4096;
#pragma unroll
    for (int gi = 0; gi < 4; ++gi) {
        float t[4];
#pragma unroll
        for (int i = 0; i < 4; ++i) t[i] = av[gi][i] + (1.f - gate) * acc[4 * gi + i];
        *(LAS u32x2*)(stg + r * 72 + gi * 16 + h * 8) = (u32x2){pk(t[0], t[1]), pk(t[2], t[3])};
    }
    char* dst = (char*)out + ((ut3 & 1) * 1024 + (ut3 >> 1) + (lane >> 3) * 32) * 896 + 56 * win + (lane & 7) * 8;
#pragma unroll
    for (int i = 0; i < 4; ++i) {
        const u32x2 v = *(LAS u32x2*)(stg + (i * 8 + (lane >> 3)) * 72 + (lane & 7) * 8);
        if ((lane & 7) != 7) *(u32x2*)(dst + i * (8 * 32 * 896)) = v;
    }
}

extern "C" void kernel_launch(void* const* d_in, const int* in_sizes, int n_in, void* d_out, int out_size, void* d_ws, size_t ws_size, hipStream_t stream) {
    fused_kernel<<<256, 512, 0, stream>>>((const u16*)d_in[0], (const u16*)d_in[4], (const u16*)d_in[6], (const u16*)d_in[1], (const u16*)d_in[2], (const u16*)d_in[5],
                                          (char*)d_ws, (u16*)d_out);
}
```
